```python
import torch

def _softmax(x, axis=-1):
    return torch.softmax(x, dim=axis)
def _gelu(x):
    return torch.nn.functional.gelu(x, approximate="tanh")
def _silu(x):
    return torch.nn.functional.silu(x)
def _sigmoid(x):
    return torch.sigmoid(x)
def _layernorm(x, axis=-1, eps=1e-5):
    m = x.mean(dim=axis, keepdim=True)
    v = x.var(dim=axis, keepdim=True, unbiased=False)
    return (x - m) / torch.sqrt(v + eps)
def _scatter_add(tgt, idx, src, axis=0):
    return tgt.clone().index_add_(axis, idx, src)
def _segment_sum(src, seg, n):
    out = torch.zeros((n,) + src.shape[1:], dtype=src.dtype)
    return out.index_add_(0, seg, src)
def _pad(x, pw):
    flat = tuple(v for pair in reversed(pw) for v in pair)
    return torch.nn.functional.pad(x, flat)

kernel_name = 'sequential_preLN__sliding_local_dilated__glu_gated_glu__prompt'

def setup_inputs():

    _w = (lambda a: (lambda t: t.to(torch.bfloat16) if t.is_floating_point() else t)(torch.as_tensor(a)))
    def _A_setup():
        B, S, D = 1, 2048, 448
        return {
            "x": _w((lambda *s: torch.randn(*s, dtype=torch.bfloat16) * 0.05)(B, S, D)),
            "wqkv": _w((lambda *s: torch.randn(*s, dtype=torch.bfloat16) * 0.05)(D, 3 * D)),
            "wo": _w((lambda *s: torch.randn(*s, dtype=torch.bfloat16) * 0.05)(D, D)),
        }
    _A_in = _A_setup()

    def _B_setup():
        B, S, D, F = 1, 2048, 448, 1792
        return {
            "x": _w((lambda *s: torch.randn(*s, dtype=torch.bfloat16) * 0.05)(B, S, D)),
            "w_gate": _w((lambda *s: torch.randn(*s, dtype=torch.bfloat16) * 0.05)(D, F)),
            "w_up": _w((lambda *s: torch.randn(*s, dtype=torch.bfloat16) * 0.05)(D, F)),
            "w_down": _w((lambda *s: torch.randn(*s, dtype=torch.bfloat16) * 0.05)(F, D)),
        }
    _B_in = _B_setup()

    return {
        "A_x": _A_in["x"],
        "A_wqkv": _A_in["wqkv"],
        "A_wo": _A_in["wo"],
        "B_x": _B_in["x"],
        "B_w_gate": _B_in["w_gate"],
        "B_w_up": _B_in["w_up"],
        "B_w_down": _B_in["w_down"],
    }

def reference(A_x, A_wqkv, A_wo, B_x, B_w_gate, B_w_up, B_w_down):
    def _A_ref(x, wqkv, wo):
        B, S, D = x.shape
        H = 16; Dh = D // H
        W, dil = 8, 2
        qkv = (lambda x, *d: x.permute(*d))((x @ wqkv).reshape(B, S, 3, H, Dh), 2, 0, 3, 1, 4)
        q, k, v = qkv[0], qkv[1], qkv[2]
        scale = Dh ** -0.5
        pos = torch.arange(S)
        off = torch.arange(W) * dil
        idx = pos[:, None] - off[None, :]
        mask = idx >= 0
        idx_c = torch.where(mask, idx, 0).reshape(-1)
        kg = k[:, :, idx_c, :].reshape(B, H, S, W, Dh)
        vg = v[:, :, idx_c, :].reshape(B, H, S, W, Dh)
        logits = torch.einsum("bhsd,bhswd->bhsw", q, kg) * scale
        logits = torch.where(mask[None, None, :, :], logits, -1e9)
        attn = _softmax(logits, axis=-1)
        o = torch.einsum("bhsw,bhswd->bhsd", attn, vg)
        return (lambda x, *d: x.permute(*d))(o, 0, 2, 1, 3).reshape(B, S, D) @ wo

    def _B_ref(x, w_gate, w_up, w_down):
        g = torch.einsum("bsd,df->bsf", x, w_gate)
        u = torch.einsum("bsd,df->bsf", x, w_up)
        h = _sigmoid(g) * u
        return torch.einsum("bsf,fd->bsd", h, w_down)

    x = A_x
    h = x + _A_ref(_layernorm(x, axis=-1), A_wqkv, A_wo)
    h2 = h + _B_ref(_layernorm(h, axis=-1), B_w_gate, B_w_up, B_w_down)
    return h2
```

```cpp
#include <hip/hip_runtime.h>
#include <hip/hip_bf16.h>
typedef unsigned short u16;
__device__ __forceinline__ float bf2f(u16 b){ return __uint_as_float(((unsigned)b)<<16); }
__device__ __forceinline__ u16 f2bf(float f){ unsigned u=__float_as_uint(f); u+=0x7fffu+((u>>16)&1u); return (u16)(u>>16); }
constexpr int S=2048,D=448,F=1792,H=16,DH=28;

__global__ void ln_k(const u16* x, u16* y){
  int row=blockIdx.x; int t=threadIdx.x;
  float v[7]; float s=0;
  for(int i=0;i<7;i++){ v[i]=bf2f(x[row*D+t+64*i]); s+=v[i]; }
  for(int o=32;o;o>>=1) s+=__shfl_xor(s,o);
  float m=s/D; float q=0;
  for(int i=0;i<7;i++){ float d=v[i]-m; q+=d*d; }
  for(int o=32;o;o>>=1) q+=__shfl_xor(q,o);
  float r=rsqrtf(q/D+1e-5f);
  for(int i=0;i<7;i++) y[row*D+t+64*i]=f2bf((v[i]-m)*r);
}
template<int MODE>
__global__ void gemm_k(const u16* A,const u16* B,const u16* B2,const u16* res,u16* C,int N,int K){
  int n=blockIdx.x*64+threadIdx.x; int m=blockIdx.y;
  __shared__ float a[1792];
  for(int k=threadIdx.x;k<K;k+=64) a[k]=bf2f(A[m*K+k]);
  __syncthreads();
  float acc=0, acc2=0;
  for(int k=0;k<K;k++){ acc+=a[k]*bf2f(B[k*N+n]); if(MODE==2) acc2+=a[k]*bf2f(B2[k*N+n]); }
  if(MODE==1) acc+=bf2f(res[m*N+n]);
  if(MODE==2) acc=acc2/(1.f+__expf(-acc));
  C[m*N+n]=f2bf(acc);
}
__global__ void attn_k(const u16* qkv,u16* o){
  int idx=blockIdx.x*64+threadIdx.x; int s=idx/H, h=idx%H;
  float l[8]; float mx=-1e30f;
  for(int w=0;w<8;w++){ int j=s-2*w; float acc=0;
    if(j>=0){ for(int d=0;d<DH;d++) acc+=bf2f(qkv[s*3*D+h*DH+d])*bf2f(qkv[j*3*D+D+h*DH+d]); acc*=rsqrtf(28.f);} else acc=-1e9f;
    l[w]=acc; mx=fmaxf(mx,acc);}
  float sum=0; for(int w=0;w<8;w++){ l[w]=__expf(l[w]-mx); sum+=l[w]; }
  for(int d=0;d<DH;d++){ float acc=0; for(int w=0;w<8;w++){ int j=s-2*w; if(j<0) j=0; acc+=l[w]*bf2f(qkv[j*3*D+2*D+h*DH+d]); } o[s*D+h*DH+d]=f2bf(acc/sum); }
}
extern "C" void kernel_launch(void* const* d_in, const int* in_sizes, int n_in, void* d_out, int out_size, void* d_ws, size_t ws_size, hipStream_t stream){
  const u16* x=(const u16*)d_in[0]; const u16* wqkv=(const u16*)d_in[1]; const u16* wo=(const u16*)d_in[2];
  const u16* wg=(const u16*)d_in[4]; const u16* wu=(const u16*)d_in[5]; const u16* wd=(const u16*)d_in[6];
  u16* ws=(u16*)d_ws; u16* xn=ws; u16* qkv=xn+S*D; u16* o=qkv+S*3*D; u16* h=o+S*D; u16* hn=h+S*D; u16* hm=hn+S*D;
  ln_k<<<S,64,0,stream>>>(x,xn);
  gemm_k<0><<<dim3(3*D/64,S),64,0,stream>>>(xn,wqkv,nullptr,nullptr,qkv,3*D,D);
  attn_k<<<S*H/64,64,0,stream>>>(qkv,o);
  gemm_k<1><<<dim3(D/64,S),64,0,stream>>>(o,wo,nullptr,x,h,D,D);
  ln_k<<<S,64,0,stream>>>(h,hn);
  gemm_k<2><<<dim3(F/64,S),64,0,stream>>>(hn,wg,wu,nullptr,hm,F,D);
  gemm_k<1><<<dim3(D/64,S),64,0,stream>>>(hm,wd,nullptr,h,(u16*)d_out,D,F);
}
```

```cpp
#include <hip/hip_runtime.h>
#include <hip/hip_bf16.h>
#include <type_traits>

#define GAS __attribute__((address_space(1)))
#define LAS __attribute__((address_space(3)))
#define DI __device__ __forceinline__
typedef unsigned v4u __attribute__((ext_vector_type(4)));
typedef unsigned v2u __attribute__((ext_vector_type(2)));
typedef float f2 __attribute__((ext_vector_type(2)));
typedef float f4 __attribute__((ext_vector_type(4)));
typedef float f16v __attribute__((ext_vector_type(16)));
typedef __bf16 b2 __attribute__((ext_vector_type(2)));
typedef __bf16 b8 __attribute__((ext_vector_type(8)));
typedef short s4 __attribute__((ext_vector_type(4)));
typedef unsigned long long u64;
typedef __amdgpu_buffer_rsrc_t rsrc_t;

constexpr int S_ = 2048, D_ = 448, F_ = 1792;
constexpr unsigned WS_F1 = 4096, WS_F2 = WS_F1 + 65536, WS_F3 = WS_F2 + 65536;
constexpr unsigned WS_RS = 512u << 10, WS_O = 1u << 20, WS_H = 3u << 20, WS_HM = 5u << 20, WS_END = 12u << 20;
constexpr unsigned WS_ST = 16u << 20;
constexpr int LDS_BYTES = 163840;
constexpr unsigned L_W1 = 0, L_HALO = 86016, L_SCR = 100352, L_WO = 100352, L_RED2 = 98304, L_DUMP = 162816;
constexpr int SPIN_CAP = 1 << 20;

template <int N, int I = 0, class F> DI void static_for(F&& f) {
  if constexpr (I < N) { f(std::integral_constant<int, I>{}); static_for<N, I + 1>(f); }
}
#define SB() __builtin_amdgcn_sched_barrier(0)
DI f4 mfma16(v4u a, v4u b, f4 c) { return __builtin_amdgcn_mfma_f32_16x16x32_bf16(__builtin_bit_cast(b8, a), __builtin_bit_cast(b8, b), c, 0, 0, 0); }
DI f16v mfma32(v4u a, v4u b, f16v c) { return __builtin_amdgcn_mfma_f32_32x32x16_bf16(__builtin_bit_cast(b8, a), __builtin_bit_cast(b8, b), c, 0, 0, 0); }
DI unsigned pk(float a, float b) { f2 v = {a, b}; b2 r = __builtin_convertvector(v, b2); return __builtin_bit_cast(unsigned, r); }
DI float lo2f(unsigned u) { return __builtin_bit_cast(float, u << 16); }
DI float hi2f(unsigned u) { return __builtin_bit_cast(float, u & 0xffff0000u); }
DI float fmx(float a, float b) { return __builtin_elementwise_maximum(a, b); }
DI float dot2(unsigned a, unsigned b, float c) { return __builtin_amdgcn_fdot2_f32_bf16(__builtin_bit_cast(b2, a), __builtin_bit_cast(b2, b), c, false); }
template <int CTRL> DI float dppf(float x) { int v = __builtin_bit_cast(int, x); int r = __builtin_amdgcn_update_dpp(v, v, CTRL, 0xf, 0xf, false); return __builtin_bit_cast(float, r); }
DI float red8(float x) { x += dppf<0xB1>(x); x += dppf<0x4E>(x); x += dppf<0x141>(x); return x; }
DI void sw16(float x, float& a, float& b) { unsigned u = __builtin_bit_cast(unsigned, x); auto r = __builtin_amdgcn_permlane16_swap(u, u, false, false); unsigned r0 = r[0], r1 = r[1]; a = __builtin_bit_cast(float, r0); b = __builtin_bit_cast(float, r1); }
DI void sw32(float x, float& a, float& b) { unsigned u = __builtin_bit_cast(unsigned, x); auto r = __builtin_amdgcn_permlane32_swap(u, u, false, false); unsigned r0 = r[0], r1 = r[1]; a = __builtin_bit_cast(float, r0); b = __builtin_bit_cast(float, r1); }

DI void dma(unsigned lds, unsigned voff, rsrc_t rs, unsigned soff) {
  asm volatile("s_mov_b32 m0, %0\n\ts_nop 0\n\tbuffer_load_dwordx4 %1, %2, %3 offen lds" ::"s"(lds), "v"(voff), "s"(rs), "s"(soff) : "memory");
}
DI void dma_sc1(unsigned lds, unsigned voff, rsrc_t rs, unsigned soff) {
  asm volatile("s_mov_b32 m0, %0\n\ts_nop 0\n\tbuffer_load_dwordx4 %1, %2, %3 offen sc1 lds" ::"s"(lds), "v"(voff), "s"(rs), "s"(soff) : "memory");
}
template <int IMM> DI v4u bload(unsigned voff, rsrc_t rs, unsigned soff) {
  v4u d; asm volatile("buffer_load_dwordx4 %0, %1, %2, %3 offen offset:%4" : "=v"(d) : "v"(voff), "s"(rs), "s"(soff), "i"(IMM) : "memory"); return d;
}
template <int IMM> DI v4u bload_sc1(unsigned voff, rsrc_t rs, unsigned soff) {
  v4u d; asm volatile("buffer_load_dwordx4 %0, %1, %2, %3 offen offset:%4 sc1" : "=v"(d) : "v"(voff), "s"(rs), "s"(soff), "i"(IMM) : "memory"); return d;
}
DI void bstore_sc1(v4u d, unsigned voff, rsrc_t rs, unsigned soff) {
  asm volatile("buffer_store_dwordx4 %0, %1, %2, %3 offen sc1\n\ts_nop 1" ::"v"(d), "v"(voff), "s"(rs), "s"(soff) : "memory");
}
template <int N> DI void waitv() { asm volatile("s_waitcnt vmcnt(%0)" ::"i"(N) : "memory"); }
template <int N> DI void waitv1(v4u& a) { asm volatile("s_waitcnt vmcnt(%1)" : "+v"(a) : "i"(N) : "memory"); }
template <int N> DI void waitv7(v4u (&a)[7]) {
  asm volatile("s_waitcnt vmcnt(%7)" : "+v"(a[0]), "+v"(a[1]), "+v"(a[2]), "+v"(a[3]), "+v"(a[4]), "+v"(a[5]), "+v"(a[6]) : "i"(N) : "memory");
}
DI void pin7(v4u (&a)[7]) { asm volatile("" : "+v"(a[0]), "+v"(a[1]), "+v"(a[2]), "+v"(a[3]), "+v"(a[4]), "+v"(a[5]), "+v"(a[6])::"memory"); }
DI void bar() { asm volatile("s_waitcnt lgkmcnt(0)\n\ts_barrier" ::: "memory"); }
DI v4u lds128(LAS char* L, unsigned off) { return *(LAS v4u*)(L + off); }
DI v4u trfrag(LAS char* L, unsigned off0, unsigned off1) {
  s4 a = __builtin_amdgcn_ds_read_tr16_b64_v4i16((LAS s4*)(L + off0));
  s4 b = __builtin_amdgcn_ds_read_tr16_b64_v4i16((LAS s4*)(L + off1));
  v2u x = __builtin_bit_cast(v2u, a), y = __builtin_bit_cast(v2u, b);
  return v4u{x.x, x.y, y.x, y.y};
}
DI void poll(GAS u64* p, bool active, u64 tag) {
#pragma unroll 1
  for (int sp = 0; sp < SPIN_CAP; ++sp) {
    u64 v = tag;
    if (active) v = __hip_atomic_load(p, __ATOMIC_RELAXED, __HIP_MEMORY_SCOPE_AGENT);
    if (__all(v == tag)) break;
    __builtin_amdgcn_s_sleep(1);
  }
}
DI void ln_group(v4u (&v)[7]) {
  float s = 0.f, s2 = 0.f;
#pragma unroll
  for (int i = 0; i < 7; ++i)
#pragma unroll
    for (int d = 0; d < 4; d += 2) { unsigned u = v[i][d], u2 = v[i][d + 1]; s = dot2(u, 0x3F803F80u, s); s2 = dot2(u2, 0x3F803F80u, s2); }
  const float mean = red8(s + s2) * (1.f / D_);
  f2 dv[28]; f2 q = {0.f, 0.f}, q2 = {0.f, 0.f};
#pragma unroll
  for (int i = 0; i < 7; ++i)
#pragma unroll
    for (int d = 0; d < 4; ++d) { unsigned u = v[i][d]; f2 t = {lo2f(u), hi2f(u)}; t -= mean; dv[4 * i + d] = t; if (d & 1) q2 += t * t; else q += t * t; }
  q += q2;
  const float rstd = __builtin_amdgcn_rsqf(red8(q.x + q.y) * (1.f / D_) + 1e-5f);
#pragma unroll
  for (int i = 0; i < 7; ++i)
#pragma unroll
    for (int d = 0; d < 4; ++d) { f2 t = dv[4 * i + d] * rstd; v[i][d] = pk(t.x, t.y); }
}
constexpr int gslot(int ks) {
  return ks < 6 ? 3 + ks : ks < 9 ? ks - 6 : ks < 12 ? ks : ks < 18 ? ks - 9 : ks < 23 ? ks - 3 : ks - 14;
}

#define STAMP(k)
#define STAMP_OUT()

__global__ void __launch_bounds__(512, 2)
fused_block(const char* __restrict__ x, const char* __restrict__ wqkv, const char* __restrict__ wo, const char* __restrict__ wg,
            const char* __restrict__ wu, const char* __restrict__ wd, char* out, char* ws) {
  extern __shared__ __attribute__((aligned(16))) char smem[];
  LAS char* L = (LAS char*)smem;
  const unsigned Lb = (unsigned)(size_t)L;
  const int tid = threadIdx.x, l = tid & 63, w = __builtin_amdgcn_readfirstlane(tid >> 6);
  const int b = blockIdx.x, xcd = b & 7, ix = b >> 3;
  STAMP(0);
  const rsrc_t rX = __builtin_amdgcn_make_buffer_rsrc((void*)x, 0, S_ * D_ * 2, 0x00020000);
  const int t1 = 4 * (xcd >> 1) + (ix >> 3), head = 8 * (xcd & 1) + (ix & 7);

  v4u xa[7], xb[7];
  {
    const int r = l >> 3, c = l & 7;
    if (w == 0) {
      u64 pc; unsigned clen;
      asm volatile("s_getpc_b64 %0\n.Lfb_pc0:\n\ts_mov_b32 %1, .Lfb_end-.Lfb_pc0" : "=s"(pc), "=s"(clen));
      const rsrc_t rC = __builtin_amdgcn_make_buffer_rsrc((void*)pc, 0, clen & ~15u, 0x00020000);
      dma(Lb + L_DUMP, (l < 12) ? (unsigned)((ix + 32 * l) * 128) : 0x40000000u, rC, 0u);
    }
    if ((w >> 1) == 1) {
      int hr = 128 * t1 - 16 + 8 * (w & 1) + r; hr = hr < 0 ? 0 : hr;
      const unsigned vc = (unsigned)(hr * 896 + 16 * (c ^ (r >> 1) ^ (4 * (w & 1))));
#pragma unroll
      for (int i = 0; i < 7; ++i) dma(Lb + L_HALO + i * 2048 + (w & 1) * 1024, vc, rX, (unsigned)(128 * i));
    }
    const unsigned va = (unsigned)((128 * t1 + 16 * w + r) * 896 + 16 * c);
    xa[0] = bload<0>(va, rX, 0u); xa[1] = bload<128>(va, rX, 0u); xa[2] = bload<256>(va, rX, 0u); xa[3] = bload<384>(va, rX, 0u);
    xa[4] = bload<512>(va, rX, 0u); xa[5] = bload<640>(va, rX, 0u); xa[6] = bload<768>(va, rX, 0u);
    xb[0] = bload<0>(va, rX, 7168u); xb[1] = bload<128>(va, rX, 7168u); xb[2] = bload<256>(va, rX, 7168u); xb[3] = bload<384>(va, rX, 7168u);
    xb[4] = bload<512>(va, rX, 7168u); xb[5] = bload<640>(va, rX, 7168u); xb[6] = bload<768>(va, rX, 7168u);
  }
  SB();
  STAMP(1);
  const rsrc_t rWS = __builtin_amdgcn_make_buffer_rsrc((void*)ws, 0, WS_END, 0x00020000);
  const bool act = ix < 28;
  const int ixc = act ? ix : 0;
  const int t64 = 4 * xcd + ixc / 7, strip = ixc % 7;
  const int t3 = 4 * (xcd >> 1) + ixc / 7, fg = 7 * (xcd & 1) + ixc % 7;
  const int kq = w >> 1, tt = w & 1;
  const int G = l >> 4, q4 = (l >> 2) & 3, p4 = l & 3, hh = l >> 5;
  unsigned l16 = (unsigned)(l * 16);
  unsigned oown = (unsigned)((64 * t64 + 8 * w + (l & 7)) * 896 + 128 * strip + 32 * ((l >> 3) & 3) + 16 * hh);
  const rsrc_t rW = __builtin_amdgcn_make_buffer_rsrc((void*)wqkv, 0, D_ * 3 * D_ * 2, 0x00020000);
  const unsigned vw = (unsigned)((l >> 2) * 2688 + 56 * head - 8 * (head & 1) + 16 * ((l & 3) ^ (2 * (l >> 5))));
  auto wissue = [&](int s, int sec) { dma(Lb + L_W1 + ((8 * s + w) * 3 + sec) * 1024, vw, rW, (unsigned)((8 * s + w) * (16 * 2688) + sec * 896)); };
  v4u xf[14], tagv, xres;
  {
    waitv7<7>(xa);
    STAMP(2);
    ln_group(xa);
    pin7(xa);
    wissue(0, 0); wissue(0, 1); wissue(0, 2);
    waitv7<3>(xb);
    ln_group(xb);
    pin7(xb);
    tagv = bload_sc1<0>(0u, rWS, 0u);
    xres = bload<0>(oown, rX, 0u);
    wissue(1, 0); wissue(1, 1); wissue(1, 2);
    const int r = l >> 3, c = l & 7;
    const unsigned wa = (unsigned)(r * 128 + ((c ^ (r >> 1)) * 16)), wb = 1024u + (wa ^ 64u);
    const int t = l & 15;
    const unsigned rd0 = (unsigned)(t * 128 + ((G ^ (t >> 1)) * 16)), rd1 = rd0 ^ 64u;
    const unsigned scr = L_SCR + w * 4096;
#pragma unroll
    for (int i = 0; i < 7; ++i) {
      const unsigned sl = scr + (i & 1) * 2048;
      *(LAS v4u*)(L + sl + wa) = xa[i];
      *(LAS v4u*)(L + sl + wb) = xb[i];
      __builtin_amdgcn_wave_barrier();
      xf[2 * i] = lds128(L, sl + rd0);
      xf[2 * i + 1] = lds128(L, sl + rd1);
      __builtin_amdgcn_wave_barrier();
    }
    if ((w >> 1) == 1) {
      v4u xc[7];
      const unsigned hb = L_HALO + (w & 1) * 1024 + l16;
#pragma unroll
      for (int i = 0; i < 7; ++i) xc[i] = lds128(L, hb + i * 2048);
      ln_group(xc);
#pragma unroll
      for (int i = 0; i < 7; ++i) *(LAS v4u*)(L + hb + i * 2048) = xc[i];
    }
  }
  f4 co, cp;
  v4u pmask;
  unsigned padl, padh, ooff;
  {
    const int t = l & 15, g = G;
    const bool first = (t1 == 0) && (w == 0);
#pragma unroll
    for (int r = 0; r < 4; ++r) {
      const int d = t - (4 * g + r);
      co[r] = (!(d & 1) && d >= 0) ? 0.f : -1e30f;
      cp[r] = (!(d & 1) && d < 0 && !first) ? 0.f : -1e30f;
    }
    const unsigned hm = (t & 1) ? 0xffff0000u : 0x0000ffffu;
    const bool own0 = t - (4 * g + (t & 1)) >= 0, own1 = t - (4 * g + 2 + (t & 1)) >= 0;
    pmask = v4u{own0 ? 0u : hm, own1 ? 0u : hm, own0 ? hm : 0u, own1 ? hm : 0u};
    padl = ((head & 1) && g == 0) ? 0u : ~0u;
    padh = (!(head & 1) && g == 3) ? 0u : ~0u;
    ooff = WS_O + (unsigned)((((4 * t1 + (w >> 1)) * 32 + 2 * head + (g >> 1)) * 64 + (g & 1) * 32 + 16 * (w & 1) + t) * 16);
  }
  const rsrc_t rGU = __builtin_amdgcn_make_buffer_rsrc((void*)(w >= 4 ? wu : wg), 0, D_ * F_ * 2, 0x00020000);
  unsigned vstrip = (unsigned)((l >> 3) * 896 + 128 * strip + 16 * ((l & 7) ^ (((l >> 4) & 1) << 2)));
  unsigned vgu = (unsigned)((l >> 3) * 3584 + 256 * fg + 128 * ((w >> 1) & 1) + 16 * ((l & 7) ^ (((l >> 4) & 1) << 2) ^ (w >= 4 ? 2 : 0)));
  unsigned gub = (unsigned)(((G & 1) * 2 + (w >> 2)) * 2048 + (4 * hh + q4) * 128 + ((((w & 3) ^ (q4 & 2) ^ (G & 1)) * 2 + (p4 >> 1)) * 16) + 8 * (p4 & 1));
  unsigned sb = (unsigned)((4 * hh + q4) * 128 + (((G & 1) * 2 + (p4 >> 1)) ^ ((q4 & 2) << 1)) * 16 + 8 * (p4 & 1));
  asm volatile("" : "+v"(co), "+v"(cp), "+v"(pmask), "+v"(padl), "+v"(padh), "+v"(ooff), "+v"(vstrip), "+v"(vgu), "+v"(gub), "+v"(sb), "+v"(l16));
  auto gu_issue = [&](int ks) { dma(Lb + gslot(ks) * 8192 + w * 1024, vgu, rGU, (unsigned)((16 * ks + 8 * (w & 1)) * 3584)); };
  auto gu_read = [&](int ks) { return trfrag(L, gub + gslot(ks) * 8192, gub + gslot(ks) * 8192 + 1024); };
  STAMP(3);
  f4 acc[6];
  {
    const rsrc_t rWo = __builtin_amdgcn_make_buffer_rsrc((void*)wo, 0, D_ * D_ * 2, 0x00020000);
    auto gemm = [&](auto HI_) {
      constexpr bool HI = HI_;
      const int g = G, q = q4, p = p4;
      const unsigned wb0 = (unsigned)((g >> 1) * 3072 + (8 * (g & 1) + q) * 64 + (((p >> 1) ^ (2 * (g & 1))) * 16) + 8 * (p & 1)), wb1 = wb0 ^ 32u;
      const unsigned hb0 = (unsigned)(L_HALO + (l & 15) * 128 + ((g ^ ((l & 15) >> 1)) * 16)), hb1 = hb0 ^ 64u;
      const unsigned wh = ((w & 1) ? wb1 : wb0) + (w >= 6 ? 2048u : 1024u);
      unsigned ub0 = wb0 + 49152, ub1 = wb1 + 49152, uh = wh + 49152;
      asm volatile("" : "+v"(ub0), "+v"(ub1), "+v"(uh));
      f4 acch = {0.f, 0.f, 0.f, 0.f};
#pragma unroll
      for (int i = 0; i < 6; ++i) acc[i] = f4{0.f, 0.f, 0.f, 0.f};
      v4u F[2][8];
      auto rd = [&](int ks, v4u(&f)[8]) {
        const unsigned b0 = ks < 8 ? wb0 : ub0, b1 = ks < 8 ? wb1 : ub1, bh = ks < 8 ? wh : uh, o = (ks & 7) * 6144;
#pragma unroll
        for (int sec = 0; sec < 3; ++sec) {
          f[2 * sec] = trfrag(L, b0 + o + sec * 1024, b0 + o + sec * 1024 + 256);
          f[2 * sec + 1] = trfrag(L, b1 + o + sec * 1024, b1 + o + sec * 1024 + 256);
        }
        if (HI) { f[6] = trfrag(L, bh + o, bh + o + 256); f[7] = lds128(L, ((ks & 1) ? hb1 : hb0) + (ks >> 1) * 2048); }
      };
      static_for<14>([&](auto KS) {
        constexpr int ks = KS, s = ks >> 2, k1 = s == 3 ? 14 : 4 * s + 4;
        if constexpr ((ks & 3) == 0) {
          if constexpr (s == 0) {}
          else if constexpr (s == 1) waitv<3>();
          else if constexpr (s == 2) waitv<HI ? 0 : 3>();
          else waitv<7>();
          bar();
          STAMP(22 + s);
          rd(ks, F[0]);
        }
        if constexpr (ks < 3) wissue(2, ks);
        if constexpr (ks >= 4 && ks < 7 && !HI) wissue(3, ks - 4);
        if constexpr (ks >= 8 && ks < 12) {
          constexpr int j = 2 * (ks - 8);
          dma(Lb + L_WO + (w + 8 * j) * 1024, vstrip, rWo, (unsigned)((w + 8 * j) * 8 * 896));
          if constexpr (j + 1 < 7) dma(Lb + L_WO + (w + 8 * j + 8) * 1024, vstrip, rWo, (unsigned)((w + 8 * j + 8) * 8 * 896));
        }
        if constexpr (ks >= 12) { gu_issue(2 * (ks - 12)); gu_issue(2 * (ks - 12) + 1); }
        SB();
        if constexpr (ks + 1 < k1) rd(ks + 1, F[(ks + 1) & 1]);
        const v4u(&f)[8] = F[ks & 1];
#pragma unroll
        for (int i = 0; i < 4; ++i) acc[i] = mfma16(f[i], xf[ks], acc[i]);
        acc[4] = mfma16(xf[ks], f[4], acc[4]);
        acc[5] = mfma16(xf[ks], f[5], acc[5]);
        if (HI) acch = mfma16(f[6], f[7], acch);
        if constexpr (ks + 1 < k1) {
#pragma unroll
          for (int i = 0; i < 7; ++i) { __builtin_amdgcn_sched_group_barrier(0x100, 2, 0); __builtin_amdgcn_sched_group_barrier(0x008, 1, 0); }
        }
        SB();
      });
      if (HI) {
        const unsigned d0 = pk(acch[0], acch[1]), d1 = pk(acch[2], acch[3]);
        if (w < 6) *(LAS v2u*)(L + l * 16 + 8 * (w & 1)) = v2u{d0, d1};
        else {
          LAS unsigned short* v = (LAS unsigned short*)(L + 1024 + (4 * g + 16 * ((l & 15) >> 2)) * 16 + 8 * (w & 1) + 2 * (l & 3));
          v[0] = (unsigned short)d0; v[8] = (unsigned short)(d0 >> 16); v[16] = (unsigned short)d1; v[24] = (unsigned short)(d1 >> 16);
        }
      }
    };
    asm volatile("s_waitcnt vmcnt(3)" : "+v"(tagv), "+v"(xres)::"memory");
    if (w >= 4) gemm(std::true_type{}); else gemm(std::false_type{});
  }
  STAMP(4);
  {
    const v4u kf = {pk(acc[2][0], acc[2][1]), pk(acc[2][2], acc[2][3]), pk(acc[3][0], acc[3][1]), pk(acc[3][2], acc[3][3])};
    const v4u vf = {pk(acc[4][0], acc[4][1]), pk(acc[4][2], acc[4][3]), pk(acc[5][0], acc[5][1]), pk(acc[5][2], acc[5][3])};
    *(LAS v4u*)(L + (w + 1) * 2048 + l16) = kf;
    *(LAS v4u*)(L + (w + 1) * 2048 + 1024 + l16) = vf;
    const float qs = 0.18898223650461362f * 1.4426950408889634f;
    const v4u qf = {pk(acc[0][0] * qs, acc[0][1] * qs) & padl, pk(acc[0][2] * qs, acc[0][3] * qs) & padl, pk(acc[1][0] * qs, acc[1][1] * qs) & padh,
                    pk(acc[1][2] * qs, acc[1][3] * qs) & padh};
    const f4 so = mfma16(kf, qf, co);
    float u0 = fmx(so[0], so[1]), u1 = fmx(so[2], so[3]);
    asm volatile("" : "+v"(u0), "+v"(u1));
    waitv<4>();
    bar();
    const v4u kp = lds128(L, w * 2048 + l16), vp = lds128(L, w * 2048 + 1024 + l16);
    gu_issue(4); gu_issue(5);
    SB();
    const f4 sp = mfma16(kp, qf, cp);
    const float v0 = fmx(fmx(sp[0], sp[1]), u0), v1 = fmx(fmx(sp[2], sp[3]), u1);
    float m = fmx(v0, v1), a, c;
    sw16(m, a, c); m = fmx(a, c);
    sw32(m, a, c); m = fmx(a, c);
    const float e0 = __builtin_amdgcn_exp2f(v0 - m), e1 = __builtin_amdgcn_exp2f(v1 - m);
    float sum = e0 + e1;
    sw16(sum, a, c); sum = a + c;
    sw32(sum, a, c); sum = a + c;
    const float rs = __builtin_amdgcn_rcpf(sum);
    const unsigned p0 = pk(e0 * rs, e0 * rs), p1 = pk(e1 * rs, e1 * rs);
    const v4u pf = {p0 & pmask.x, p1 & pmask.y, p0 & pmask.z, p1 & pmask.w};
    const f4 z = {0.f, 0.f, 0.f, 0.f};
    const f4 o0 = mfma16(v4u{vp.x, vp.y, vf.x, vf.y}, pf, z), o1 = mfma16(v4u{vp.z, vp.w, vf.z, vf.w}, pf, z);
    const v4u of = {pk(o0[0], o0[1]) & padl, pk(o0[2], o0[3]) & padl, pk(o1[0], o1[1]) & padh, pk(o1[2], o1[3]) & padh};
    bstore_sc1(of, ooff, rWS, 0u);
  }
  STAMP(5);
  SB();
  GAS char* wsg = (GAS char*)ws;
  u64 tag = (((u64)tagv.y << 32) | tagv.x) * 0x9E3779B97F4A7C15ull + 0x632BE59BD9B4E019ull;
  v4u W2[16];
  {
    const unsigned o = L_WO + sb + (unsigned)(kq * 112 * 128);
#pragma unroll
    for (int n = 0; n < 8; ++n) {
      const unsigned ro = (unsigned)((28 * (n >> 1) - 4 * ((n >> 1) & 1) + 8 * (n & 1)) * 128);
      W2[2 * n] = trfrag(L, o + ro, o + ro + 2048);
      W2[2 * n + 1] = trfrag(L, (o ^ 64u) + ro, (o ^ 64u) + ro + 2048);
    }
  }
  asm volatile("" : "+v"(tag));
  waitv<0>();
  bar();
  STAMP(6);
  if (tid == 0) __hip_atomic_store((GAS u64*)(wsg + WS_F1 + (t1 * 16 + head) * 256), tag, __ATOMIC_RELAXED, __HIP_MEMORY_SCOPE_AGENT);
  if (!act) {
    if (b == 255 && w < 4) {
      poll((GAS u64*)(wsg + WS_F1 + tid * 256), true, tag);
      bar();
      if (tid == 0) __hip_atomic_store((GAS u64*)wsg, tag, __ATOMIC_RELAXED, __HIP_MEMORY_SCOPE_AGENT);
    }
    STAMP_OUT();
    return;
  }
  v4u W3[28];
  f16v cs;
#pragma unroll
  for (int i = 0; i < 16; ++i) cs[i] = 0.f;
  const v4u ones = {0x3F803F80u, 0x3F803F80u, 0x3F803F80u, 0x3F803F80u};
#pragma unroll
  for (int ks = 0; ks < 6; ++ks) W3[ks] = gu_read(ks);
#pragma unroll
  for (int ks = 6; ks < 12; ++ks) gu_issue(ks);
#pragma unroll
  for (int ks = 0; ks < 6; ++ks) cs = mfma32(W3[ks], ones, cs);
  SB();
  if (w == 0) poll((GAS u64*)(wsg + WS_F1 + ((t64 >> 1) * 16 + (l & 15)) * 256), l < 16, tag);
  waitv<0>();
  bar();
  STAMP(7);
  v4u hres;
  const unsigned rown = (unsigned)((((l >> 3) & 3) * 2 + (w >> 2)) * 2048), lown = (unsigned)(((8 * (w & 3) + (l & 7) + 32 * hh) ^ ((l >> 1) & 8)) * 16);
  {
    v4u of[8];
    const unsigned so = WS_O + (unsigned)(((2 * t64 + tt) * 32 + 8 * kq) * 1024);
    of[0] = bload_sc1<0>(l16, rWS, so); of[1] = bload_sc1<1024>(l16, rWS, so); of[2] = bload_sc1<2048>(l16, rWS, so); of[3] = bload_sc1<3072>(l16, rWS, so);
    of[4] = bload_sc1<0>(l16, rWS, so + 4096); of[5] = bload_sc1<1024>(l16, rWS, so + 4096); of[6] = bload_sc1<2048>(l16, rWS, so + 4096); of[7] = bload_sc1<3072>(l16, rWS, so + 4096);
#pragma unroll
    for (int ks = 6; ks < 12; ++ks) W3[ks] = gu_read(ks);
    auto r0 = __builtin_amdgcn_permlane32_swap(xres.x, xres.z, false, false);
    auto r1 = __builtin_amdgcn_permlane32_swap(xres.y, xres.w, false, false);
    const unsigned x0 = r0[0], x2 = r0[1], x1 = r1[0], x3 = r1[1];
    f4 own0 = {lo2f(x0), hi2f(x0), lo2f(x1), hi2f(x1)}, own1 = {lo2f(x2), hi2f(x2), lo2f(x3), hi2f(x3)};
    unsigned rrd = L_RED2 + 4 * rown + lown, rw0 = L_RED2 + (unsigned)((tt * 4 + kq) * 2048) + l16, rw1 = rw0 ^ 128u;
    unsigned hoff = WS_H + (unsigned)((((2 * t64 + (w >> 2)) * 28 + 4 * strip + ((l >> 3) & 3)) * 64 + 32 * hh + 8 * (w & 3) + (l & 7)) * 16);
    asm volatile("" : "+v"(own0), "+v"(own1), "+v"(rrd), "+v"(rw0), "+v"(rw1), "+v"(hoff));
    STAMP(8);
    f16v a0, a1;
#pragma unroll
    for (int i = 0; i < 16; ++i) { a0[i] = 0.f; a1[i] = 0.f; }
    static_for<8>([&](auto N_) {
      constexpr int n = N_;
      waitv1<7 - n + (n > 2 ? n - 2 : 0)>(of[n]);
      a0 = mfma32(W2[2 * n], of[n], a0); a1 = mfma32(W2[2 * n + 1], of[n], a1);
      SB();
      if constexpr (n >= 2) gu_issue(10 + n);
    });
    STAMP(9);
#pragma unroll
    for (int j = 0; j < 4; ++j) {
      const f16v& a = (j >> 1) ? a1 : a0;
      const unsigned o = ((j >> 1) ? rw1 : rw0) + j * 16384;
      *(LAS f4*)(L + o) = f4{a[8 * (j & 1)], a[8 * (j & 1) + 1], a[8 * (j & 1) + 2], a[8 * (j & 1) + 3]};
      *(LAS f4*)(L + o + 1024) = f4{a[8 * (j & 1) + 4], a[8 * (j & 1) + 5], a[8 * (j & 1) + 6], a[8 * (j & 1) + 7]};
    }
    bar();
    f4 pr[8];
#pragma unroll
    for (int s = 0; s < 8; ++s) pr[s] = *(LAS f4*)(L + rrd + s * 1024);
    SB();
    own0 += (pr[0] + pr[2]) + (pr[4] + pr[6]);
    own1 += (pr[1] + pr[3]) + (pr[5] + pr[7]);
    hres = v4u{pk(own0[0], own0[1]), pk(own0[2], own0[3]), pk(own1[0], own1[1]), pk(own1[2], own1[3])};
    bstore_sc1(hres, hoff, rWS, 0u);
    float s1 = 0.f, s2 = 0.f;
#pragma unroll
    for (int d = 0; d < 4; ++d) { const unsigned u = hres[d]; s1 = dot2(u, 0x3F803F80u, s1); s2 = dot2(u, u, s2); }
    s1 += dppf<0x128>(s1); s2 += dppf<0x128>(s2);
    float a, c;
    sw16(s1, a, c); s1 = a + c;
    sw16(s2, a, c); s2 = a + c;
    sw32(s1, a, c); s1 = a + c;
    sw32(s2, a, c); s2 = a + c;
    if (l < 8) __builtin_amdgcn_raw_buffer_store_b64(v2u{__builtin_bit_cast(unsigned, s1), __builtin_bit_cast(unsigned, s2)}, rWS,
                                                     WS_RS + (unsigned)(((t64 * 7 + strip) * 64 + 8 * w + l) * 8), 0, 16);
  }
  STAMP(10);
  SB();
#pragma unroll
  for (int ks = 6; ks < 12; ++ks) cs = mfma32(W3[ks], ones, cs);
  SB();
  waitv<0>();
  bar();
  STAMP(11);
  if (tid == 0) __hip_atomic_store((GAS u64*)(wsg + WS_F2 + (t64 * 7 + strip) * 256), tag, __ATOMIC_RELAXED, __HIP_MEMORY_SCOPE_AGENT);
#pragma unroll
  for (int ks = 12; ks < 18; ++ks) W3[ks] = gu_read(ks);
#pragma unroll
  for (int ks = 18; ks < 28; ++ks) gu_issue(ks);
#pragma unroll
  for (int ks = 12; ks < 18; ++ks) cs = mfma32(W3[ks], ones, cs);
  SB();
  if (w == 0) poll((GAS u64*)(wsg + WS_F2 + (t3 * 14 + (l & 15)) * 256), l < 14, tag);
  waitv<0>();
  bar();
  STAMP(12);
  constexpr unsigned L_RS = 114688;
  constexpr unsigned L_WD = 122880;
  const unsigned sh = WS_H + (unsigned)(4 * t3 * 28 * 1024);
  auto h_issue = [&](int j) { dma_sc1(Lb + (w + 8 * j) * 1024, l16, rWS, sh + (w + 8 * j) * 1024); };
#pragma unroll
  for (int j = 0; j < 7; ++j) h_issue(j);
  dma_sc1(Lb + L_RS + w * 1024, l16, rWS, WS_RS + (unsigned)(t3 * 7168 + w * 1024));
#pragma unroll
  for (int ks = 18; ks < 28; ++ks) W3[ks] = gu_read(ks);
#pragma unroll
  for (int ks = 18; ks < 28; ++ks) cs = mfma32(W3[ks], ones, cs);
  SB();
  const rsrc_t rWd = __builtin_amdgcn_make_buffer_rsrc((void*)wd, 0, F_ * D_ * 2, 0x00020000);
  auto wd_slot = [&](int r) { const int i = 8 * r + w; return (unsigned)(i < 20 ? L_WD + i * 2048 : (i - 20) * 2048); };
  auto wd_issue = [&](int r, int h) { dma(Lb + wd_slot(r % 7) + h * 1024, vstrip, rWd, (unsigned)((128 * r + 16 * w + 8 * h) * 896)); };
  v4u W4[28];
  auto wd_read = [&](int r) {
    const unsigned o = wd_slot(r % 7) + sb;
    W4[2 * r] = trfrag(L, o, o + 1024); W4[2 * r + 1] = trfrag(L, o ^ 64u, (o ^ 64u) + 1024);
  };
  {
    constexpr int RD = 7;
    constexpr int BS = 16;
    unsigned lhi = l16 + 57344;
    unsigned str = L_RS + (unsigned)((l & 31) * 8);
    asm volatile("" : "+v"(lhi), "+v"(str));
    waitv<4>();
    bar();
    STAMP(13);
    auto hfrag = [&](int n) { return n < 56 ? lds128(L, l16 + n * 1024) : lds128(L, lhi + (n - 56) * 1024); };
    v4u ring[RD];
#pragma unroll
    for (int i = 0; i < RD; ++i) ring[i] = hfrag(i);
    SB();
    f16v ac[2];
    f2 sp[4];
    float s3, s4, mean, sd, ge, eo[8], et;
    const unsigned shm = WS_HM + (unsigned)((4 * t3 * 112 + 8 * fg + w) * 1024);
    auto fin_read = [&](int t, int s) { return *(LAS f2*)(L + str + ((t >> 1) * 7 + s) * 512 + (t & 1) * 256); };
    auto epi_a = [&](const f16v& a, int i) { et = __builtin_amdgcn_exp2f((a[i] - mean * cs[i]) * ge) * sd + sd; };
    auto epi_b = [&](const f16v& a, int i) { eo[i] = (a[i + 8] - mean * cs[i + 8]) * __builtin_amdgcn_rcpf(et); };
    auto epi_st = [&](int t) {
      const v4u r = {pk(eo[0], eo[1]), pk(eo[2], eo[3]), pk(eo[4], eo[5]), pk(eo[6], eo[7])};
      bstore_sc1(r, l16, rWS, shm + (unsigned)(t * 112 * 1024));
    };
    static_for<112>([&](auto N_) {
      constexpr int n = N_, t = n / 28, i = n % 28;
      f16v& a = ac[t & 1];
      if constexpr (i == 0) {
#pragma unroll
        for (int k = 0; k < 16; ++k) a[k] = 0.f;
      }
      a = mfma32(W3[i], ring[i % RD], a);
      if constexpr (n + RD < 112) ring[i % RD] = hfrag(n + RD);
      if constexpr (t > 0 && i >= 2 && i < 18) {
        if constexpr (i & 1) { epi_b(ac[(t - 1) & 1], (i - 2) >> 1); asm volatile("" : "+v"(eo[(i - 2) >> 1])); }
        else { epi_a(ac[(t - 1) & 1], (i - 2) >> 1); asm volatile("" : "+v"(et)); }
      }
      if constexpr (t > 0 && i == 18) epi_st(t - 1);
      if constexpr (i == 19) { sp[0] = fin_read(t, 0); sp[1] = fin_read(t, 1); sp[2] = fin_read(t, 2); sp[3] = fin_read(t, 3); }
      if constexpr (i == 21) { s3 = (sp[0].x + sp[1].x) + (sp[2].x + sp[3].x); asm volatile("" : "+v"(s3)); }
      if constexpr (i == 22) {
        s4 = (sp[0].y + sp[1].y) + (sp[2].y + sp[3].y); asm volatile("" : "+v"(s4));
        sp[0] = fin_read(t, 4); sp[1] = fin_read(t, 5); sp[2] = fin_read(t, 6);
      }
      if constexpr (i == 24) { mean = ((s3 + sp[0].x) + (sp[1].x + sp[2].x)) * (1.f / D_); asm volatile("" : "+v"(mean)); }
      if constexpr (i == 25) { s4 = ((s4 + sp[0].y) + (sp[1].y + sp[2].y)) * (1.f / D_); asm volatile("" : "+v"(s4)); }
      if constexpr (i == 26) { s4 = fmx(s4 - mean * mean, 0.f) + 1e-5f; s3 = __builtin_amdgcn_rsqf(s4); asm volatile("" : "+v"(s3), "+v"(s4)); }
      if constexpr (i == 27) { sd = s4 * s3; ge = -1.4426950408889634f * s3; asm volatile("" : "+v"(sd), "+v"(ge)); }
      if constexpr (t == 0 && i < 7) h_issue(7 + i);
      if constexpr (t == 0 && i >= 8 && i < 12) wd_issue((i - 8) >> 1, i & 1);
      if constexpr ((t == 1 || t == 2) && i > BS && i <= BS + 4) wd_issue(2 * t + ((i - BS - 1) >> 1), (i - BS - 1) & 1);
      if constexpr (t == 3 && i > BS && i <= BS + 2) wd_issue(6, i - BS - 1);
      if constexpr (t == 3 && i == 2) waitv<0>();
      if constexpr (t == 3 && i >= 2 && i < 14 && !(i & 1)) wd_read((i - 2) >> 1);
      if constexpr (t == 3 && i >= 5 && i < 17) {
        if constexpr (i & 1) asm volatile("" : "+v"(W4[i - 5]), "+v"(W4[i - 4]));
        wd_issue(7 + ((i - 5) >> 1), (i - 5) & 1);
      }
      SB();
      if constexpr (i == BS) {
        if constexpr (t == 0) waitv<11>();
        if constexpr (t == 1) waitv<4>();
        asm volatile("s_barrier" ::: "memory");
        SB();
      }
    });
    STAMP(15);
#pragma unroll
    for (int i = 0; i < 8; ++i) { epi_a(ac[1], i); epi_b(ac[1], i); }
    epi_st(3);
  }
  STAMP(16);
  SB();
  waitv<1>();
#pragma unroll
  for (int r = 6; r < 13; ++r) wd_read(r);
  asm volatile("s_waitcnt lgkmcnt(0)" ::: "memory");
  wd_issue(13, 0); wd_issue(13, 1);
  waitv<2>();
  bar();
  STAMP(17);
  if (tid == 0) __hip_atomic_store((GAS u64*)(wsg + WS_F3 + (t3 * 14 + fg) * 256), tag, __ATOMIC_RELAXED, __HIP_MEMORY_SCOPE_AGENT);
  if (w == 0) poll((GAS u64*)(wsg + WS_F3 + ((t64 >> 1) * 14 + (l & 15)) * 256), l < 14, tag);
  waitv<0>();
  wd_read(13);
  bar();
  STAMP(19);
  {
    constexpr int HR = 20;
    const unsigned sm = WS_HM + (unsigned)((2 * t64 * 112 + w) * 1024);
    v4u hf[HR];
#pragma unroll
    for (int n = 0; n < HR; ++n) hf[n] = bload_sc1<0>(l16, rWS, sm + (unsigned)((8 * n) * 1024));
    unsigned rw0 = (unsigned)(w * 2048) + l16, rw1 = (rw0 ^ 128u) + 65536u;
    asm volatile("" : "+v"(rw0), "+v"(rw1));
    f16v a0, a1;
    static_for<28>([&](auto N_) {
      constexpr int n = N_, t = n / 14, k = n % 14;
      if constexpr (k == 0) {
#pragma unroll
        for (int i = 0; i < 16; ++i) { a0[i] = 0.f; a1[i] = 0.f; }
      }
      waitv1<(n + HR < 28 ? HR - 1 : 27 - n)>(hf[n % HR]);
      a0 = mfma32(W4[2 * k], hf[n % HR], a0); a1 = mfma32(W4[2 * k + 1], hf[n % HR], a1);
      SB();
      if constexpr (n + HR < 28) hf[n % HR] = bload_sc1<0>(l16, rWS, sm + (unsigned)((8 * (n + HR)) * 1024));
      if constexpr (k == 13) {
#pragma unroll
        for (int j = 0; j < 4; ++j) {
          const f16v& a = (j >> 1) ? a1 : a0;
          const unsigned o = ((j >> 1) ? rw1 : rw0) + ((j & 1) * 2 + t) * 16384;
          *(LAS f4*)(L + o) = f4{a[8 * (j & 1)], a[8 * (j & 1) + 1], a[8 * (j & 1) + 2], a[8 * (j & 1) + 3]};
          *(LAS f4*)(L + o + 1024) = f4{a[8 * (j & 1) + 4], a[8 * (j & 1) + 5], a[8 * (j & 1) + 6], a[8 * (j & 1) + 7]};
        }
        SB();
      }
    });
    STAMP(20);
    f4 o0 = {lo2f(hres.x), hi2f(hres.x), lo2f(hres.y), hi2f(hres.y)}, o1 = {lo2f(hres.z), hi2f(hres.z), lo2f(hres.w), hi2f(hres.w)};
    unsigned rrd = 8 * rown + lown;
    asm volatile("" : "+v"(o0), "+v"(o1), "+v"(rrd));
    bar();
    f4 pr[16];
#pragma unroll
    for (int s = 0; s < 16; ++s) pr[s] = *(LAS f4*)(L + rrd + s * 1024);
    SB();
    o0 += ((pr[0] + pr[2]) + (pr[4] + pr[6])) + ((pr[8] + pr[10]) + (pr[12] + pr[14]));
    o1 += ((pr[1] + pr[3]) + (pr[5] + pr[7])) + ((pr[9] + pr[11]) + (pr[13] + pr[15]));
    auto r0 = __builtin_amdgcn_permlane32_swap(pk(o0[0], o0[1]), pk(o1[0], o1[1]), false, false);
    auto r1 = __builtin_amdgcn_permlane32_swap(pk(o0[2], o0[3]), pk(o1[2], o1[3]), false, false);
    const v4u r = {r0[0], r1[0], r0[1], r1[1]};
    *(GAS v4u*)((GAS char*)out + oown) = r;
  }
  STAMP(21);
  STAMP_OUT();
  asm volatile(".Lfb_end:" ::: "memory");
}

extern "C" void kernel_launch(void* const* d_in, const int* in_sizes, int n_in, void* d_out, int out_size, void* d_ws, size_t ws_size,
                              hipStream_t stream) {
  (void)hipFuncSetAttribute((const void*)fused_block, hipFuncAttributeMaxDynamicSharedMemorySize, LDS_BYTES);
  fused_block<<<256, 512, LDS_BYTES, stream>>>((const char*)d_in[0], (const char*)d_in[1], (const char*)d_in[2], (const char*)d_in[4],
                                               (const char*)d_in[5], (const char*)d_in[6], (char*)d_out, (char*)d_ws);
}
```

```cpp
#define FT 1
#include <hip/hip_runtime.h>
#include <hip/hip_bf16.h>
#include <type_traits>

#define GAS __attribute__((address_space(1)))
#define LAS __attribute__((address_space(3)))
#define DI __device__ __forceinline__
typedef unsigned v4u __attribute__((ext_vector_type(4)));
typedef unsigned v2u __attribute__((ext_vector_type(2)));
typedef float f2 __attribute__((ext_vector_type(2)));
typedef float f4 __attribute__((ext_vector_type(4)));
typedef float f16v __attribute__((ext_vector_type(16)));
typedef __bf16 b2 __attribute__((ext_vector_type(2)));
typedef __bf16 b8 __attribute__((ext_vector_type(8)));
typedef short s4 __attribute__((ext_vector_type(4)));
typedef unsigned long long u64;
typedef __amdgpu_buffer_rsrc_t rsrc_t;

constexpr int S_ = 2048, D_ = 448, F_ = 1792;
constexpr unsigned WS_F1 = 640u << 10, WS_F2 = 4096, WS_F3 = WS_F2 + 65536, WS_F4 = WS_F3 + 65536, WS_F5 = WS_F4 + 65536, WS_CS = 448u << 10;
constexpr unsigned WS_RS = 512u << 10, WS_O = 1u << 20, WS_H = 3u << 20, WS_HM = 5u << 20, WS_END = 12u << 20;
constexpr unsigned WS_ST = 16u << 20;
constexpr int LDS_BYTES = 163840;
constexpr unsigned L_WR = 71680, L_WO = 40960, L_XK = 137216, L_XQ = 147456, L_RED2 = 40960, L_DUMP = 162816, L_TB = 161792, L_CONS = L_TB + 640, L_CNT = L_TB + 656;
constexpr int SPIN_CAP = 1 << 20;

template <int N, int I = 0, class F> DI void static_for(F&& f) {
  if constexpr (I < N) { f(std::integral_constant<int, I>{}); static_for<N, I + 1>(f); }
}
#define SB() __builtin_amdgcn_sched_barrier(0)
DI f4 mfma16(v4u a, v4u b, f4 c) { return __builtin_amdgcn_mfma_f32_16x16x32_bf16(__builtin_bit_cast(b8, a), __builtin_bit_cast(b8, b), c, 0, 0, 0); }
DI f16v mfma32(v4u a, v4u b, f16v c) { return __builtin_amdgcn_mfma_f32_32x32x16_bf16(__builtin_bit_cast(b8, a), __builtin_bit_cast(b8, b), c, 0, 0, 0); }
DI unsigned pk(float a, float b) { f2 v = {a, b}; b2 r = __builtin_convertvector(v, b2); return __builtin_bit_cast(unsigned, r); }
DI float lo2f(unsigned u) { return __builtin_bit_cast(float, u << 16); }
DI float hi2f(unsigned u) { return __builtin_bit_cast(float, u & 0xffff0000u); }
DI float fmx(float a, float b) { return __builtin_elementwise_maximum(a, b); }
DI float dot2(unsigned a, unsigned b, float c) { return __builtin_amdgcn_fdot2_f32_bf16(__builtin_bit_cast(b2, a), __builtin_bit_cast(b2, b), c, false); }
template <int CTRL> DI float dppf(float x) { int v = __builtin_bit_cast(int, x); int r = __builtin_amdgcn_update_dpp(0, v, CTRL, 0xf, 0xf, true); return __builtin_bit_cast(float, r); }
DI float red8(float x) { x += dppf<0xB1>(x); x += dppf<0x4E>(x); x += dppf<0x141>(x); return x; }
DI void sw16(float x, float& a, float& b) { unsigned u = __builtin_bit_cast(unsigned, x); auto r = __builtin_amdgcn_permlane16_swap(u, u, false, false); unsigned r0 = r[0], r1 = r[1]; a = __builtin_bit_cast(float, r0); b = __builtin_bit_cast(float, r1); }
DI void sw32(float x, float& a, float& b) { unsigned u = __builtin_bit_cast(unsigned, x); auto r = __builtin_amdgcn_permlane32_swap(u, u, false, false); unsigned r0 = r[0], r1 = r[1]; a = __builtin_bit_cast(float, r0); b = __builtin_bit_cast(float, r1); }

DI void dma(unsigned lds, unsigned voff, rsrc_t rs, unsigned soff) {
  asm volatile("s_mov_b32 m0, %0\n\ts_nop 0\n\tbuffer_load_dwordx4 %1, %2, %3 offen lds" ::"s"(lds), "v"(voff), "s"(rs), "s"(soff) : "memory");
}
DI void dma_sc1(unsigned lds, unsigned voff, rsrc_t rs, unsigned soff) {
  asm volatile("s_mov_b32 m0, %0\n\ts_nop 0\n\tbuffer_load_dwordx4 %1, %2, %3 offen sc1 lds" ::"s"(lds), "v"(voff), "s"(rs), "s"(soff) : "memory");
}
template <int IMM> DI v4u bload(unsigned voff, rsrc_t rs, unsigned soff) {
  v4u d; asm volatile("buffer_load_dwordx4 %0, %1, %2, %3 offen offset:%4" : "=v"(d) : "v"(voff), "s"(rs), "s"(soff), "i"(IMM) : "memory"); return d;
}
template <int IMM> DI v4u bload_sc1(unsigned voff, rsrc_t rs, unsigned soff) {
  v4u d; asm volatile("buffer_load_dwordx4 %0, %1, %2, %3 offen offset:%4 sc1" : "=v"(d) : "v"(voff), "s"(rs), "s"(soff), "i"(IMM) : "memory"); return d;
}
DI void bstore_sc1(v4u d, unsigned voff, rsrc_t rs, unsigned soff) {
  asm volatile("buffer_store_dwordx4 %0, %1, %2, %3 offen sc1\n\ts_nop 1" ::"v"(d), "v"(voff), "s"(rs), "s"(soff) : "memory");
}
template <int N> DI void waitv() { asm volatile("s_waitcnt vmcnt(%0)" ::"i"(N) : "memory"); }
template <int N> DI void waitv1(v4u& a) { asm volatile("s_waitcnt vmcnt(%1)" : "+v"(a) : "i"(N) : "memory"); }
template <int N> DI void waitv7(v4u (&a)[7]) {
  asm volatile("s_waitcnt vmcnt(%7)" : "+v"(a[0]), "+v"(a[1]), "+v"(a[2]), "+v"(a[3]), "+v"(a[4]), "+v"(a[5]), "+v"(a[6]) : "i"(N) : "memory");
}
DI void pin7(v4u (&a)[7]) { asm volatile("" : "+v"(a[0]), "+v"(a[1]), "+v"(a[2]), "+v"(a[3]), "+v"(a[4]), "+v"(a[5]), "+v"(a[6])::"memory"); }
DI void bar() { asm volatile("s_waitcnt lgkmcnt(0)\n\ts_barrier" ::: "memory"); }
DI v4u lds128(LAS char* L, unsigned off) { return *(LAS v4u*)(L + off); }
DI v4u trfrag(LAS char* L, unsigned off0, unsigned off1) {
  s4 a = __builtin_amdgcn_ds_read_tr16_b64_v4i16((LAS s4*)(L + off0));
  s4 b = __builtin_amdgcn_ds_read_tr16_b64_v4i16((LAS s4*)(L + off1));
  v2u x = __builtin_bit_cast(v2u, a), y = __builtin_bit_cast(v2u, b);
  return v4u{x.x, x.y, y.x, y.y};
}
DI void poll(GAS u64* p, bool active, u64 tag) {
#pragma unroll 1
  for (int sp = 0; sp < SPIN_CAP; ++sp) {
    u64 v = tag;
    if (active) v = __hip_atomic_load(p, __ATOMIC_RELAXED, __HIP_MEMORY_SCOPE_AGENT);
    if (__all(v == tag)) break;
    __builtin_amdgcn_s_sleep(1);
  }
}
typedef int v4i __attribute__((ext_vector_type(4)));
DI void lds_wait(LAS char* L, unsigned off, int need) {
  for (;;) {
    const v4i v = *(volatile LAS v4i*)(L + off);
    if (__builtin_amdgcn_readfirstlane(min(min(v.x, v.y), min(v.z, v.w))) >= need) break;
    __builtin_amdgcn_s_sleep(1);
  }
  asm volatile("" ::: "memory");
}
DI void lds_post(LAS char* L, unsigned off, int v) { asm volatile("s_waitcnt lgkmcnt(0)" ::: "memory"); *(volatile LAS int*)(L + off) = v; }
DI void arrive(LAS char* L, unsigned off, unsigned n, GAS u64* flag, u64 tag, int l) {
  if (l == 0 && __hip_atomic_fetch_add((LAS unsigned*)(L + off), 1u, __ATOMIC_RELAXED, __HIP_MEMORY_SCOPE_WORKGROUP) == n - 1)
    __hip_atomic_store(flag, tag, __ATOMIC_RELAXED, __HIP_MEMORY_SCOPE_AGENT);
}
DI float red32(float x) { x = red8(x); x += dppf<0x140>(x); float a, c; sw16(x, a, c); return a + c; }
#define PG0 3
#define PG1 8
#define PGA 14
constexpr int G0 = PG0, G1 = PG1, GA = PGA, GC = 24;
constexpr int gslot(int ks) { return ks >= GC ? ks - 9 : ks >= 14 ? ks - 10 : ks < 5 ? ks : ks < 12 ? 7 + ks : ks - 3; }
static_assert(GA == 14 && G1 <= 12, "slots");

#define STAMP(k)
#define STAMP_OUT()

__global__ void __launch_bounds__(512, 2)
fused_block(const char* __restrict__ x, const char* __restrict__ wqkv, const char* __restrict__ wo, const char* __restrict__ wg,
            const char* __restrict__ wu, const char* __restrict__ wd, char* out, char* ws) {
  extern __shared__ __attribute__((aligned(16))) char smem[];
  LAS char* L = (LAS char*)smem;
  const unsigned Lb = (unsigned)(size_t)L;
  const int tid = threadIdx.x, l = tid & 63, w = __builtin_amdgcn_readfirstlane(tid >> 6);
  const int b = blockIdx.x, xcd = b & 7, ix = b >> 3;
  STAMP(0);
  const rsrc_t rX = __builtin_amdgcn_make_buffer_rsrc((void*)x, 0, S_ * D_ * 2, 0x00020000);
  const int t1 = 8 * (xcd >> 1) + (ix >> 2), hp = 4 * (xcd & 1) + (ix & 3);
  const rsrc_t rW = __builtin_amdgcn_make_buffer_rsrc((void*)wqkv, 0, D_ * 3 * D_ * 2, 0x00020000);

  {
    const int r = l >> 3, c = l & 7;
    if (w < 4) {
      const unsigned ve = (unsigned)((64 * t1 + 16 * w + r) * 896 + 16 * (c ^ (r >> 1)));
#pragma unroll
      for (int i = 0; i < 7; ++i) {
        const int p = (i + 2 * (ix & 3)) % 7;
        dma(Lb + (w + 1) * 2048 + p * 10240, ve, rX, (unsigned)(128 * p));
        dma(Lb + (w + 1) * 2048 + p * 10240 + 1024, ve ^ 64u, rX, (unsigned)(128 * p + 7168));
      }
    } else {
      if (w == 7) {
        u64 pc; unsigned clen;
        asm volatile("s_getpc_b64 %0\n.Lfb_pc0:\n\ts_mov_b32 %1, .Lfb_end-.Lfb_pc0" : "=s"(pc), "=s"(clen));
        const rsrc_t rC = __builtin_amdgcn_make_buffer_rsrc((void*)pc, 0, clen & ~15u, 0x00020000);
        dma(Lb + L_DUMP, (l < 12) ? (unsigned)((ix + 32 * l) * 128) : 0x40000000u, rC, 0u);
        dma(Lb + L_DUMP, (unsigned)((b + 256 * l) * 128), __builtin_amdgcn_make_buffer_rsrc((void*)wo, 0, D_ * D_ * 2, 0x00020000), 0u);
      }
      if (w == 5) dma_sc1(Lb + L_DUMP, l < 4 ? WS_F1 + (unsigned)((4 * b + l) * 256) : l < 8 ? WS_F2 + (unsigned)((l - 4) * 65536 + b * 256) : l < 12 ? WS_RS + (unsigned)((4 * b + l - 8) * 128) :
           0x40000000u, __builtin_amdgcn_make_buffer_rsrc((void*)ws, 0, WS_END, 0x00020000), 0u);
      if (w == 6) dma(Lb + L_DUMP, (unsigned)((b + 256 * l) * 128), rW, 0u);
      const unsigned ve = (unsigned)(((t1 > 0 ? 64 * t1 - 16 : 0) + 8 * (w & 1) + r) * 896 + 16 * (c ^ (r >> 1) ^ (4 * (w & 1))));
#pragma unroll
      for (int j = 0; j < 4; ++j) {
        const int p = ((w >> 1) & 1) + 2 * j;
        if (p < 7) dma(Lb + (w & 1) * 1024 + p * 10240, ve, rX, (unsigned)(128 * p));
      }
    }
  }
  SB();
  unsigned vw;
  {
    const int r = l >> 3, c = (l & 7) ^ (2 * (((r >> 1) & 1) | ((w & 1) << 1)));
    vw = c == 7 ? 0x40000000u : (unsigned)((8 * (w & 3) + r) * 2688 + 112 * hp + 16 * c);
  }
  auto wissue = [&](int u0, int u1) {
#pragma unroll
    for (int u = u0; u < u1; ++u)
      dma(Lb + L_WR + ((u + 6) & 15) * 4096 + (w & 3) * 1024, vw, rW, (unsigned)(32 * (u < 28 ? u >> 1 : (u - 21) % 14) * 2688 + (u < 28 ? u & 1 : 2) * 896));
  };
  if (w >= 4) wissue(0, 12);
  SB();
  STAMP(1);
  const rsrc_t rWS = __builtin_amdgcn_make_buffer_rsrc((void*)ws, 0, WS_END, 0x00020000);
  const bool act = ix < 28;
  const int ixc = act ? ix : 0;
  const int t64 = 4 * xcd + ixc / 7, strip = ixc % 7;
  const int tb = 4 * (t64 >> 1) + (t64 & 1);
  const int t3 = 8 * (xcd >> 2) + ((28 * (xcd & 3) + ixc) & 7), fg = (28 * (xcd & 3) + ixc) >> 3;
  const int G = l >> 4, q4 = (l >> 2) & 3, p4 = l & 3, hh = l >> 5;
  unsigned l16 = (unsigned)(l * 16);
  const rsrc_t rWo = __builtin_amdgcn_make_buffer_rsrc((void*)wo, 0, D_ * D_ * 2, 0x00020000);
  const rsrc_t rWd = __builtin_amdgcn_make_buffer_rsrc((void*)wd, 0, F_ * D_ * 2, 0x00020000);
  const rsrc_t rGU = __builtin_amdgcn_make_buffer_rsrc((void*)((w & 2) ? wu : wg), 0, D_ * F_ * 2, 0x00020000);
  v4u tagv, xres;
  unsigned oown, vstrip, vgu;
  auto consts = [&]() {
    int lz = l;
    asm volatile("" : "+v"(lz));
    oown = (unsigned)((32 * (tb + 2 * (w >> 2)) + 8 * (w & 3) + (lz & 7)) * 896 + 128 * strip + 32 * ((lz >> 3) & 3) + 16 * (lz >> 5));
    vstrip = (unsigned)((lz >> 3) * 896 + 128 * strip + 16 * ((lz & 7) ^ (((lz >> 4) & 1) << 2)));
    vgu = (unsigned)((lz >> 3) * 3584 + 256 * fg + 128 * (w & 1) + 16 * ((lz & 7) ^ (((lz >> 4) & 1) << 2) ^ (w & 2)));
    asm volatile("" : "+v"(oown), "+v"(vstrip), "+v"(vgu));
  };
  auto late = [&]() { tagv = bload_sc1<0>(0u, rWS, 0u); xres = bload<0>(oown, rX, 0u); };
  auto wait0 = [&]() { asm volatile("s_waitcnt vmcnt(0)" : "+v"(tagv), "+v"(xres)::"memory"); };
  auto wo_issue = [&](int n) { dma(Lb + L_WO + n * 1024, vstrip, rWo, (unsigned)(n * 8 * 896)); };
  auto row_stats = [&](int gx) {
    LAS float* tb_ = (LAS float*)(L + L_TB);
    v4u xs[7];
#pragma unroll
    for (int i = 0; i < 7; ++i) xs[i] = lds128(L, gx * 1024 + i * 10240 + l16);
    float s1[2] = {0.f, 0.f}, s2[2] = {0.f, 0.f};
#pragma unroll
    for (int i = 0; i < 7; ++i)
#pragma unroll
      for (int d = 0; d < 4; ++d) { const unsigned u = xs[i][d]; s1[d & 1] = dot2(u, 0x3F803F80u, s1[d & 1]); s2[d & 1] = dot2(u, u, s2[d & 1]); }
    const float m0 = red8(s1[0] + s1[1]) * (1.f / D_);
    const float r0 = __builtin_amdgcn_rsqf(fmx(red8(s2[0] + s2[1]) * (1.f / D_) - m0 * m0, 0.f) + 1e-5f);
    tb_[8 * gx + (l >> 3)] = m0; tb_[80 + 8 * gx + (l >> 3)] = r0;
  };
#define RBAR() asm volatile("s_barrier" ::: "memory")
  if (w >= 4) {
    waitv<8>();
    if (!(w & 1) && l < (w == 4 ? 32 : 24)) *(LAS v4u*)(L + (((w >> 1) & 1) + 2 * (l >> 3)) * 10240 + (l & 7) * 16) = v4u{0x3F803F80u, 0x3F803F80u, 0x3F803F80u, 0x3F803F80u};
    bar();
    f4 co, cp;
    v4u pmask;
    unsigned rk0, rk1;
    static_for<8>([&](auto J_) {
      constexpr int j = J_;
      constexpr int c0 = j < 5 ? 12 + 3 * j : 4 * j + 8, c1 = j < 4 ? 15 + 3 * j : j < 7 ? 4 * j + 12 : 42;
      wissue(c0, c1);
      int lz = l;
      asm volatile("" : "+v"(lz));
      const int t = lz & 15, g = lz >> 4;
      if constexpr (j == 0) { if (w < 6) row_stats(w & 1); }
      if constexpr (j == 1) consts();
      if constexpr (j == 2) late();
      if constexpr (j == 3) {
#pragma unroll
        for (int r = 0; r < 4; ++r) {
          const int d = t - (4 * g + r);
          co[r] = (!(d & 1) && d >= 0) ? 0.f : -1e30f;
          cp[r] = (!(d & 1) && d < 0) ? 0.f : -1e30f;
        }
        asm volatile("" : "+v"(co), "+v"(cp));
      }
      if constexpr (j == 4) {
        const unsigned hm = (t & 1) ? 0xffff0000u : 0x0000ffffu;
        const bool own0 = t - (4 * g + (t & 1)) >= 0, own1 = t - (4 * g + 2 + (t & 1)) >= 0;
        pmask = v4u{own0 ? 0u : hm, own1 ? 0u : hm, own0 ? hm : 0u, own1 ? hm : 0u};
        rk0 = L_TB + 320 + (unsigned)((4 * g + (t & 1) + (own0 ? 16 : 0) + 32 * (w & 1)) * 4);
        rk1 = L_TB + 320 + (unsigned)((4 * g + 2 + (t & 1) + (own1 ? 16 : 0) + 32 * (w & 1)) * 4);
        asm volatile("" : "+v"(pmask), "+v"(rk0), "+v"(rk1));
      }
      if constexpr (j == 2 || j == 3) waitv<c1 - 4 * j - 8 + (j == 2 ? 2 : 1)>();
      else if constexpr (j == 4) asm volatile("s_waitcnt vmcnt(4)" : "+v"(tagv), "+v"(xres)::"memory");
      else if constexpr (j == 7) waitv<2>();
      else waitv<c1 - 4 * j - 8>();
      if constexpr (j == 0) bar(); else RBAR();
    });
    {
      const unsigned row = (unsigned)(14 * ix + 7 * (w & 1) + (l >> 3));
      dma(Lb + L_DUMP, l < 56 ? row * 3584 + (unsigned)(((7 * (xcd & 3)) >> 1) * 256 + (l & 7) * 128) : 0x40000000u, rGU, 0u);
    }
    const unsigned xq = L_XQ + (unsigned)((w & 3) * 2048) + l16, xk = L_XK + (unsigned)((((w >> 1) & 1) * 5 + 2 * (w & 1)) * 1024) + l16;
    v4u qf[2], kf[3];
    float q0[2], q1[2];
#pragma unroll
    for (int u = 0; u < 2; ++u) { qf[u] = lds128(L, xq + u * 1024); q0[u] = *(LAS float*)(L + rk0 + 64 * u); q1[u] = *(LAS float*)(L + rk1 + 64 * u); }
#pragma unroll
    for (int u = 0; u < 3; ++u) kf[u] = lds128(L, xk + u * 1024);
    __builtin_amdgcn_s_setprio(2);
    float e0[2], e1[2];
#pragma unroll
    for (int u = 0; u < 2; ++u) {
      f4 cpu = cp;
      if (u == 0 && t1 == 0 && !(w & 1)) cpu = f4{-1e30f, -1e30f, -1e30f, -1e30f};
      const f4 so = mfma16(kf[u + 1], qf[u], co), sp = mfma16(kf[u], qf[u], cpu);
      const float v0 = fmx(fmx(sp[0], sp[1]), fmx(so[0], so[1])), v1 = fmx(fmx(sp[2], sp[3]), fmx(so[2], so[3]));
      float m = fmx(v0, v1), a, c;
      sw16(m, a, c); m = fmx(a, c);
      sw32(m, a, c); m = fmx(a, c);
      e0[u] = __builtin_amdgcn_exp2f(v0 - m); e1[u] = __builtin_amdgcn_exp2f(v1 - m);
    }
#pragma unroll
    for (int u = 0; u < 2; ++u) {
      float sum = e0[u] + e1[u], a, c;
      sw16(sum, a, c); sum = a + c;
      sw32(sum, a, c); sum = a + c;
      const float rs = __builtin_amdgcn_rcpf(sum);
      const float f0 = e0[u] * rs * q0[u], f1 = e1[u] * rs * q1[u];
      const unsigned p0 = pk(f0, f0), p1 = pk(f1, f1);
      *(LAS v4u*)(L + xq + u * 1024) = v4u{p0 & pmask.x, p1 & pmask.y, p0 & pmask.z, p1 & pmask.w};
    }
    __builtin_amdgcn_s_setprio(0);
    waitv<1>();
    bar();
  } else {
    const int ct = w & 1, hg = w >> 1;
    consts();
    unsigned wb0 = L_WR + (unsigned)(G * 1024 + q4 * 128 + (((3 * hg + 2 * ct + (p4 >> 1)) ^ (2 * ((q4 >> 1) | ((G & 1) << 1)))) * 16) + 8 * (p4 & 1));
    unsigned xr0 = (unsigned)((l & 15) * 128 + ((G ^ ((l & 15) >> 1)) * 16)), xr1 = xr0 ^ 64u, xr2 = xr0 + 61440, xr3 = xr1 + 61440;
    unsigned xw = (unsigned)(8 * ct) + l16;
    unsigned padq = ((ct ? (!hg && G == 3) : (hg && G == 0))) ? 0u : ~0u;
    unsigned ooff = WS_O + (unsigned)(((2 * t1 * 32 + 2 * (2 * hp + hg) + ct) * 64 + 32 * (G >> 1) + 16 * (G & 1) + (l & 15)) * 16);
    asm volatile("" : "+v"(wb0), "+v"(xr0), "+v"(xr1), "+v"(xr2), "+v"(xr3), "+v"(xw), "+v"(padq), "+v"(ooff));
    if (tid < 8) *(LAS int*)(L + L_CONS + 4 * tid) = 0;
    waitv<0>();
    STAMP(2);
    {
      LAS float* tb_ = (LAS float*)(L + L_TB);
      v4u xs[2][7];
#pragma unroll
      for (int i = 0; i < 14; ++i) xs[i & 1][i >> 1] = lds128(L, (2 * w + 2 + (i & 1)) * 1024 + (i >> 1) * 10240 + l16);
      SB();
      float s1[2] = {0.f, 0.f}, s2[2] = {0.f, 0.f};
#pragma unroll
      for (int i = 0; i < 7; ++i)
#pragma unroll
        for (int d = 0; d < 4; ++d)
#pragma unroll
          for (int k = 0; k < 2; ++k) { const unsigned u = xs[k][i][d]; s1[k] = dot2(u, 0x3F803F80u, s1[k]); s2[k] = dot2(u, u, s2[k]); }
#pragma unroll
      for (int k = 0; k < 2; ++k) {
        const float m0 = red8(s1[k]) * (1.f / D_);
        const float r0 = __builtin_amdgcn_rsqf(fmx(red8(s2[k]) * (1.f / D_) - m0 * m0, 0.f) + 1e-5f);
        tb_[16 * w + 16 + 8 * k + (l >> 3)] = m0; tb_[96 + 16 * w + 8 * k + (l >> 3)] = r0;
      }
    }
    bar();
    STAMP(22);
    late();
    f4 acc[3][5];
#pragma unroll
    for (int i = 0; i < 15; ++i) acc[i / 5][i % 5] = f4{0.f, 0.f, 0.f, 0.f};
    v4u F[2][2], X[14][5], pf[4];
    f4 mv[5];
    auto rdw = [&](int d) {
#pragma unroll
      for (int j = 0; j < 2; ++j) F[d & 1][j] = trfrag(L, wb0 + ((2 * d + j + 6) & 15) * 4096, wb0 + ((2 * d + j + 6) & 15) * 4096 + 512);
    };
    auto rdx = [&](int ks) {
      const unsigned xb = ks < 12 ? ((ks & 1) ? xr1 : xr0) : ((ks & 1) ? xr3 : xr2), xo = ks < 12 ? (ks >> 1) * 10240 : 0;
#pragma unroll
      for (int i = 0; i < 5; ++i) X[ks][i] = lds128(L, xb + xo + i * 2048);
    };
    rdw(0); rdx(0);
    static_for<21>([&](auto D_s) {
      constexpr int d = D_s;
      if constexpr ((d & 1) && d < 17) {
        if constexpr (d == 15) bar(); else RBAR();
        if constexpr (d == 15) STAMP(3);
      }
      if constexpr (d == 20) {
        bar();
        STAMP(24);
        rdw(20);
#pragma unroll
        for (int i = 0; i < 4; ++i) pf[i] = lds128(L, L_XQ + (unsigned)((hg * 4 + i) * 1024) + l16);
      }
      SB();
      if constexpr (d < 19) rdw(d + 1);
      if constexpr (d < 13) rdx(d + 1);
      if constexpr (d >= 15 && d < 19) { rdx(2 * d - 30); if constexpr (d < 18) rdx(2 * d - 29); }
      if constexpr (d < 14) {
#pragma unroll
        for (int i = 0; i < 5; ++i) { acc[0][i] = mfma16(F[d & 1][0], X[d][i], acc[0][i]); acc[1][i] = mfma16(F[d & 1][1], X[d][i], acc[1][i]); }
      } else {
#pragma unroll
        for (int j = 0; j < 2; ++j)
#pragma unroll
          for (int i = 0; i < 5; ++i) acc[2][i] = mfma16(X[(2 * d + j - 21) % 14][i], F[d & 1][j], acc[2][i]);
      }
      if constexpr (d < 19) {
#pragma unroll
        for (int i = 0; i < 10; ++i) {
          __builtin_amdgcn_sched_group_barrier(0x100, d >= 15 ? 2 : 1, 0); __builtin_amdgcn_sched_group_barrier(0x008, 1, 0);
        }
      }
      SB();
      if constexpr (d == 13) {
        float mt[5], rt[5];
#pragma unroll
        for (int i = 0; i < 5; ++i) { mt[i] = *(LAS float*)(L + L_TB + 64 * i + 4 * (l & 15)); rt[i] = *(LAS float*)(L + L_TB + 320 + 64 * i + 4 * (l & 15)); }
        f4 cq, ck;
#pragma unroll
        for (int r = 0; r < 4; ++r) { cq[r] = dppf<0x150>(acc[0][0][r]); ck[r] = dppf<0x150>(acc[1][0][r]); }
#pragma unroll
        for (int i = 0; i < 5; ++i) {
          const f4 k = (acc[1][i] - ck * mt[i]) * rt[i];
          *(LAS v2u*)(L + L_XK + (hg * 5 + i) * 1024 + xw) = v2u{pk(k[0], k[1]), pk(k[2], k[3])};
          if (i > 0) {
            const f4 q = (acc[0][i] - cq * mt[i]) * (rt[i] * (0.18898223650461362f * 1.4426950408889634f));
            *(LAS v2u*)(L + L_XQ + (hg * 4 + i - 1) * 1024 + xw) = v2u{pk(q[0], q[1]) & padq, pk(q[2], q[3]) & padq};
          }
        }
        SB();
      }
      if constexpr (d == 18) {
        wait0();
#pragma unroll
        for (int i = 0; i < 5; ++i) { mv[i] = *(LAS f4*)(L + L_TB + 64 * i + 16 * G); asm volatile("" : "+v"(mv[i])); }
        SB();
      }
    });
    STAMP(4);
    float cv, z_;
    sw16(acc[2][0][0], cv, z_); sw32(cv, cv, z_);
    v2u vb[5];
#pragma unroll
    for (int i = 0; i < 5; ++i) { const f4 v = acc[2][i] - mv[i] * cv; vb[i] = v2u{pk(v[0], v[1]), pk(v[2], v[3])}; }
    const f4 z = {0.f, 0.f, 0.f, 0.f};
    v2u ob[4];
#pragma unroll
    for (int i = 0; i < 4; ++i) {
      const f4 o = mfma16(v4u{vb[i].x, vb[i].y, vb[i + 1].x, vb[i + 1].y}, pf[i], z);
      ob[i] = v2u{pk(o[0], o[1]) & padq, pk(o[2], o[3]) & padq};
    }
#pragma unroll
    for (int i = 0; i < 2; ++i) {
      auto r0 = __builtin_amdgcn_permlane16_swap(ob[2 * i].x, ob[2 * i + 1].x, false, false);
      auto r1 = __builtin_amdgcn_permlane16_swap(ob[2 * i].y, ob[2 * i + 1].y, false, false);
      bstore_sc1(v4u{r0[0], r1[0], r0[1], r1[1]}, ooff, rWS, (unsigned)(i * 32768));
    }
  }
  STAMP(5);
  GAS char* wsg = (GAS char*)ws;
  u64 tag = (((u64)tagv.y << 32) | tagv.x) * 0x9E3779B97F4A7C15ull + 0x632BE59BD9B4E019ull;
  asm volatile("" : "+v"(tag));
  waitv<0>();
  if (w < 4) {
    if (l == 0) __hip_atomic_store((GAS u64*)(wsg + WS_F1 + ((t1 * 8 + hp) * 4 + w) * 256), tag, __ATOMIC_RELAXED, __HIP_MEMORY_SCOPE_AGENT);
    STAMP(23);
  } else if (act) {
#pragma unroll
    for (int j = 0; j < 14; ++j) wo_issue(w - 4 + 4 * j);
  }
  SB();
  auto gu_issue = [&](int k0, int k1) {
#pragma unroll
    for (int ks = k0; ks < k1; ++ks)
#pragma unroll
      for (int j = 0; j < 2; ++j) dma(Lb + gslot(ks) * 8192 + (w & 3) * 2048 + j * 1024, vgu, rGU, (unsigned)((16 * ks + 8 * j) * 3584));
  };
  unsigned gub = (unsigned)(((G & 1) * 2 + (w >> 2)) * 2048 + (4 * hh + q4) * 128 + ((((w & 3) ^ (q4 & 2) ^ (G & 1)) * 2 + (p4 >> 1)) * 16) + 8 * (p4 & 1));
  unsigned sb = (unsigned)((4 * hh + q4) * 128 + (((G & 1) * 2 + (p4 >> 1)) ^ ((q4 & 2) << 1)) * 16 + 8 * (p4 & 1));
  auto gu_read = [&](int ks) { return trfrag(L, gub + gslot(ks) * 8192, gub + gslot(ks) * 8192 + 1024); };
  const unsigned rown = (unsigned)((((l >> 3) & 3) * 2 + (w >> 2)) * 2048), lown = (unsigned)(((8 * (w & 3) + (l & 7) + 32 * hh) ^ ((l >> 1) & 8)) * 16);
  f4 own0, own1;
  unsigned rrd = L_RED2 + 2 * rown + lown;
  unsigned hoff = WS_H + (unsigned)((((tb + 2 * (w >> 2)) * 28 + 4 * strip + ((l >> 3) & 3)) * 64 + 32 * hh + 8 * (w & 3) + (l & 7)) * 16);
  unsigned soff = l < 8 ? WS_RS + (unsigned)((((tb + 2 * (w >> 2)) * 7 + strip) * 32 + 8 * (w & 3) + l) * 8) : 0x40000000u;
  {
    auto r0 = __builtin_amdgcn_permlane32_swap(xres.x, xres.z, false, false);
    auto r1 = __builtin_amdgcn_permlane32_swap(xres.y, xres.w, false, false);
    const unsigned x0 = r0[0], x2 = r0[1], x1 = r1[0], x3 = r1[1];
    own0 = f4{lo2f(x0), hi2f(x0), lo2f(x1), hi2f(x1)}; own1 = f4{lo2f(x2), hi2f(x2), lo2f(x3), hi2f(x3)};
    asm volatile("" : "+v"(own0), "+v"(own1), "+v"(rrd), "+v"(hoff), "+v"(soff), "+v"(gub), "+v"(sb));
  }
  if (w >= 4 && act) {
    gu_issue(0, G0);
    waitv<2 * G0>();
  }
  bar();
  STAMP(6);
  if (!act) {
    const int u = 8 * (ix - 28) + xcd;
    if (u < 28) {
      const GAS char* src = (const GAS char*)(u < 14 ? wg : wu) + (tid >> 4) * 3584 + (u % 14) * 256 + (tid & 15) * 16;
      v4u v[14];
#pragma unroll
      for (int i = 0; i < 14; ++i) v[i] = *(const GAS v4u*)(src + i * 32 * 3584);
      float a[8];
#pragma unroll
      for (int d = 0; d < 4; ++d) {
        a[2 * d] = 0.f; a[2 * d + 1] = 0.f;
#pragma unroll
        for (int i = 0; i < 14; ++i) { const unsigned x_ = v[i][d]; a[2 * d] += lo2f(x_); a[2 * d + 1] += hi2f(x_); }
      }
      waitv<0>();
      bar();
      *(LAS f4*)(L + (tid >> 4) * 512 + (tid & 15) * 32) = f4{a[0], a[1], a[2], a[3]};
      *(LAS f4*)(L + (tid >> 4) * 512 + (tid & 15) * 32 + 16) = f4{a[4], a[5], a[6], a[7]};
      bar();
      if (tid < 128) {
        float z = 0.f;
#pragma unroll
        for (int i = 0; i < 32; ++i) z += *(LAS float*)(L + i * 512 + tid * 4);
        __builtin_amdgcn_raw_buffer_store_b32(__builtin_bit_cast(unsigned, z), rWS, WS_CS + (unsigned)(u * 512 + tid * 4), 0, 16);
      }
      waitv<0>();
      bar();
      if (tid == 0) __hip_atomic_store((GAS u64*)(wsg + WS_F5 + u * 256), tag, __ATOMIC_RELAXED, __HIP_MEMORY_SCOPE_AGENT);
    }
    {
#pragma unroll 1
      for (int j = 0; j < 7; ++j) dma(Lb + L_DUMP, (unsigned)(((j * 8 + w) * 4 + (ix - 28)) * 8192 + l * 128), rWd, 0u);
    }
    if (b == 255 && w < 4) {
      poll((GAS u64*)(wsg + WS_F1 + tid * 1024), true, tag);
      bar();
      if (tid == 0) __hip_atomic_store((GAS u64*)wsg, tag, __ATOMIC_RELAXED, __HIP_MEMORY_SCOPE_AGENT);
    }
    STAMP_OUT();
    return;
  }
  if (w >= 4) {
    gu_issue(G0, G1);
    lds_wait(L, L_CONS, 2);
    gu_issue(G1, GA);
    waitv<0>();
  } else {
    const int kh = w >> 1, tt = w & 1;
    v4u W2[32];
    {
      const unsigned o = L_WO + sb + (unsigned)(kh * 224 * 128 + hh * 512);
#pragma unroll
      for (int n = 0; n < 16; ++n) {
        const unsigned ro = (unsigned)((28 * (n >> 1) - 4 * ((n >> 1) & 1) + 16 * (n & 1)) * 128);
        W2[2 * n] = trfrag(L, o + ro, o + ro + 512);
        W2[2 * n + 1] = trfrag(L, (o ^ 64u) + ro, (o ^ 64u) + ro + 512);
      }
    }
    unsigned rw0 = L_RED2 + (unsigned)((tt * 2 + kh) * 2048) + l16, rw1 = rw0 ^ 128u;
    asm volatile("" : "+v"(rw0), "+v"(rw1));
    lds_post(L, L_CONS + 4 * w, 1);
    poll((GAS u64*)(wsg + WS_F1 + (((2 * (t64 >> 1) + tt) * 8 + 4 * kh) * 4 + (l & 15)) * 256), l < 16, tag);
    STAMP(7);
    constexpr int OR = 12;
    v4u of[OR];
    const unsigned so = WS_O + (unsigned)(((tb + 2 * tt) * 32 + 16 * kh) * 1024);
    static_for<OR>([&](auto N_) { constexpr int n = N_; of[n] = bload_sc1<(n & 3) * 1024>(l16, rWS, so + (n >> 2) * 4096); });
    lds_wait(L, L_CONS, 1);
    STAMP(8);
    f16v a0, a1;
#pragma unroll
    for (int i = 0; i < 16; ++i) { a0[i] = 0.f; a1[i] = 0.f; }
    static_for<16>([&](auto N_) {
      constexpr int n = N_;
      waitv1<(n + OR < 16 ? OR - 1 : 15 - n)>(of[n % OR]);
      a0 = mfma32(W2[2 * n], of[n % OR], a0); a1 = mfma32(W2[2 * n + 1], of[n % OR], a1);
      SB();
      if constexpr (n + OR < 16) of[n % OR] = bload_sc1<((n + OR) & 3) * 1024>(l16, rWS, so + ((n + OR) >> 2) * 4096);
      if constexpr (n + OR == 15) lds_post(L, L_CONS + 4 * w, 2);
    });
    STAMP(9);
#pragma unroll
    for (int j = 0; j < 4; ++j) {
      const f16v& a = (j >> 1) ? a1 : a0;
      const unsigned o = ((j >> 1) ? rw1 : rw0) + j * 8192;
      *(LAS f4*)(L + o) = f4{a[8 * (j & 1)], a[8 * (j & 1) + 1], a[8 * (j & 1) + 2], a[8 * (j & 1) + 3]};
      *(LAS f4*)(L + o + 1024) = f4{a[8 * (j & 1) + 4], a[8 * (j & 1) + 5], a[8 * (j & 1) + 6], a[8 * (j & 1) + 7]};
    }
  }
  bar();
  v4u hres, W3[28];
  {
    f4 pr[4];
#pragma unroll
    for (int s = 0; s < 4; ++s) pr[s] = *(LAS f4*)(L + rrd + s * 1024);
    SB();
    own0 += pr[0] + pr[2];
    own1 += pr[1] + pr[3];
    hres = v4u{pk(own0[0], own0[1]), pk(own0[2], own0[3]), pk(own1[0], own1[1]), pk(own1[2], own1[3])};
    bstore_sc1(hres, hoff, rWS, 0u);
    float s1 = 0.f, s2 = 0.f;
#pragma unroll
    for (int d = 0; d < 4; ++d) { const unsigned u = hres[d]; s1 = dot2(u, 0x3F803F80u, s1); s2 = dot2(u, u, s2); }
    s1 += dppf<0x128>(s1); s2 += dppf<0x128>(s2);
    float a, c;
    sw16(s1, a, c); s1 = a + c;
    sw16(s2, a, c); s2 = a + c;
    sw32(s1, a, c); s1 = a + c;
    sw32(s2, a, c); s2 = a + c;
    __builtin_amdgcn_raw_buffer_store_b64(v2u{__builtin_bit_cast(unsigned, s1), __builtin_bit_cast(unsigned, s2)}, rWS, soff, 0, 16);
  }
  STAMP(10);
  SB();
#pragma unroll
  for (int ks = 0; ks < GA; ++ks) W3[ks] = gu_read(ks);
  bar();
  if (w >= 4) { gu_issue(GA, GA + 2); waitv<4>(); } else waitv<0>();
  arrive(L, L_CNT + 4, 8, (GAS u64*)(wsg + WS_F2 + (t64 * 7 + strip) * 256), tag, l);
  STAMP(11);
  if (w >= 4) gu_issue(GA + 2, GC);
  if (w == 0) poll((GAS u64*)(wsg + WS_F2 + (t3 * 14 + (l & 15)) * 256), l < 14, tag);
  waitv<0>();
  bar();
  STAMP(12);
  constexpr unsigned L_RS = 114688;
  constexpr unsigned L_WD = 122880;
  const unsigned sh = WS_H + (unsigned)(4 * t3 * 28 * 1024);
  auto h_issue = [&](int j) { dma_sc1(Lb + (w + 8 * j) * 1024, l16, rWS, sh + (w + 8 * j) * 1024); };
#pragma unroll
  for (int j = 0; j < 4; ++j) h_issue(j);
  dma_sc1(Lb + L_RS + w * 1024, l16, rWS, WS_RS + (unsigned)(t3 * 7168 + w * 1024));
#pragma unroll
  for (int ks = GA; ks < GC; ++ks) W3[ks] = gu_read(ks);
  if (w >= 4) gu_issue(GC, 28);
  v4u csv[4];
  v2u f5v;
  SB();
  auto wsl = [](int r) { return r < 4 ? r : r < 6 ? r - 4 : r < 8 ? r - 2 : r < 10 ? r - 6 : r < 12 ? r - 10 : r == 12 ? 6 : 4; };
  auto wd_slot = [&](int sl) { const int i = 8 * sl + w; return (unsigned)(i < 20 ? L_WD + i * 2048 : (i - 20) * 2048); };
  auto wd_issue = [&](int r, int h) { dma(Lb + wd_slot(wsl(r)) + h * 1024, vstrip, rWd, (unsigned)((128 * r + 16 * w + 8 * h) * 896)); };
  v4u W4[28];
  auto wd_read = [&](int r) {
    const unsigned o = wd_slot(wsl(r)) + sb;
    W4[2 * r] = trfrag(L, o, o + 1024); W4[2 * r + 1] = trfrag(L, o ^ 64u, (o ^ 64u) + 1024);
  };
  v2u f3v;
  {
    constexpr int RD = 5;
    unsigned lhi = l16 + 57344;
    unsigned str = L_RS + (unsigned)(hh * 1792 + (l & 31) * 8);
    asm volatile("" : "+v"(lhi), "+v"(str));
    if (w >= 4) waitv<2 * (28 - GC)>(); else waitv<0>();
    bar();
    STAMP(13);
    if (w >= 4) {
#pragma unroll
      for (int j = 0; j < 6; ++j) dma_sc1(Lb + (28 + w + 4 * j) * 1024, l16, rWS, sh + (28 + w + 4 * j) * 1024);
    }
    auto hfrag = [&](int n) { return n < 56 ? lds128(L, l16 + n * 1024) : lds128(L, lhi + (n - 56) * 1024); };
    v4u ring[RD];
#pragma unroll
    for (int i = 0; i < RD; ++i) ring[i] = hfrag(i);
    SB();
    f16v ac[2];
    f2 sp[4];
    float s3, s4, mn[2], vr[2], mean, sd, ge, eo[8], et[8];
    const unsigned shm = WS_HM + (unsigned)((4 * t3 * 112 + 8 * fg + w) * 1024);
    auto cs = [&](int j) { const unsigned u = csv[j >> 2][j & 3]; return __builtin_bit_cast(float, u); };
    auto fin_read = [&](int p, int s) { return *(LAS f2*)(L + str + p * 3584 + s * 256); };
    auto epi_st = [&](int t) {
      const v4u r = {pk(eo[0], eo[1]), pk(eo[2], eo[3]), pk(eo[4], eo[5]), pk(eo[6], eo[7])};
      bstore_sc1(r, l16, rWS, shm + (unsigned)(t * 112 * 1024));
    };
#define PINF(x) asm volatile("" : "+v"(x))
#define PINW(r) asm volatile("" : "+v"(W4[2 * (r)]), "+v"(W4[2 * (r) + 1]))
    static_for<112>([&](auto N_) {
      constexpr int n = N_, t = n / 28, i = n % 28;
      f16v& a = ac[t & 1];
      if constexpr (i == 0) {
#pragma unroll
        for (int k = 0; k < 16; ++k) a[k] = 0.f;
      }
      a = mfma32(W3[i], ring[n % RD], a);
      if constexpr (n + RD < 112) ring[n % RD] = hfrag(n + RD);
      if constexpr (t > 0) {
        const f16v& e = ac[(t - 1) & 1];
        static_for<8>([&](auto J_) {
          constexpr int j = J_, d = i - 2 - 2 * j;
          if constexpr (d == 0) { et[j] = e[j] - mean * cs(j); PINF(et[j]); }
          if constexpr (d == 1) { et[j] *= ge; PINF(et[j]); }
          if constexpr (d == 2) { et[j] = __builtin_amdgcn_exp2f(et[j]); PINF(et[j]); }
          if constexpr (d == 3) { et[j] = et[j] * sd + sd; PINF(et[j]); }
          if constexpr (d == 4) { eo[j] = e[j + 8] - mean * cs(j + 8); PINF(eo[j]); }
          if constexpr (d == 5) { et[j] = __builtin_amdgcn_rcpf(et[j]); PINF(et[j]); }
          if constexpr (d == 6) { eo[j] *= et[j]; PINF(eo[j]); }
        });
        if constexpr (i == 23) epi_st(t - 1);
      }
      if constexpr (t == 0 && i >= 2 && i < 22) {
        constexpr int p = (i - 2) / 10, d = (i - 2) % 10;
        if constexpr (d == 0) { sp[0] = fin_read(p, 0); sp[1] = fin_read(p, 1); sp[2] = fin_read(p, 2); sp[3] = fin_read(p, 3); }
        if constexpr (d == 2) { s3 = sp[0].x + sp[1].x; PINF(s3); s4 = sp[0].y + sp[1].y; PINF(s4); mn[p] = sp[2].x + sp[3].x; PINF(mn[p]); }
        if constexpr (d == 3) {
          vr[p] = sp[2].y + sp[3].y; PINF(vr[p]); s3 += mn[p]; PINF(s3);
          sp[0] = fin_read(p, 4); sp[1] = fin_read(p, 5); sp[2] = fin_read(p, 6);
        }
        if constexpr (d == 5) { s4 += vr[p]; PINF(s4); s3 += sp[0].x; PINF(s3); mn[p] = sp[1].x + sp[2].x; PINF(mn[p]); }
        if constexpr (d == 6) { s4 += sp[0].y; PINF(s4); vr[p] = sp[1].y + sp[2].y; PINF(vr[p]); s3 += mn[p]; PINF(s3); }
        if constexpr (d == 7) { s4 += vr[p]; PINF(s4); mn[p] = s3 * (1.f / D_); PINF(mn[p]); }
        if constexpr (d == 8) { s4 *= (1.f / D_); PINF(s4); s4 -= mn[p] * mn[p]; PINF(s4); }
        if constexpr (d == 9) { vr[p] = fmx(s4, 0.f) + 1e-5f; PINF(vr[p]); }
      }
      if constexpr (t == 0 && i >= 20 && i - 20 + GC < 28) W3[i - 20 + GC] = gu_read(i - 20 + GC);
      if constexpr (i == 24) { float x, y; sw32(mn[t >> 1], x, y); mean = (t & 1) ? y : x; PINF(mean); }
      if constexpr (i == 25) { float x, y; sw32(vr[t >> 1], x, y); s4 = (t & 1) ? y : x; PINF(s4); }
      if constexpr (i == 26) { s3 = __builtin_amdgcn_rsqf(s4); PINF(s3); }
      if constexpr (i == 27) { sd = s4 * s3; ge = -1.4426950408889634f * s3; PINF(sd); PINF(ge); }
      if constexpr (t == 0 && i >= 6 && i < 27 && i % 3 == 0) h_issue(5 + i / 3);
      if constexpr (t == 1 && i >= 3 && i < 27 && i % 3 == 0) wd_issue((i - 3) / 6, ((i - 3) / 3) & 1);
      if constexpr (t == 2 && i == 2) { waitv<5>(); wd_read(0); }
      if constexpr (t == 2 && i == 5) wd_read(1);
      if constexpr (t == 2 && i >= 3 && i < 15 && i % 3 == 0) wd_issue(6 + (i - 3) / 6, ((i - 3) / 3) & 1);
      if constexpr (t == 2 && i >= 15 && i < 27 && i % 3 == 0) {
        if constexpr (i == 15) { PINW(0); PINW(1); }
        wd_issue(4 + (i - 15) / 6, ((i - 15) / 3) & 1);
      }
      if constexpr (t == 3 && i == 1) wd_read(2);
      if constexpr (t == 3 && i == 4) wd_read(3);
      if constexpr (t == 3 && i == 8) wd_read(6);
      if constexpr (t == 3 && i == 14) wd_read(4);
      if constexpr (t == 3 && i == 16) wd_read(5);
      if constexpr (t == 3 && i == 22) wd_read(7);
      if constexpr (t == 3 && i >= 5 && (i & 1)) {
        constexpr int k = (i - 5) >> 2, h = ((i - 5) >> 1) & 1;
        constexpr int job = k == 0 ? 8 : k == 1 ? 9 : k == 2 ? 13 : k == 3 ? 12 : k == 4 ? 10 : 11;
        if constexpr (h == 0 && k == 0) PINW(2);
        if constexpr (h == 0 && k == 1) PINW(3);
        if constexpr (h == 0 && k == 2) PINW(6);
        if constexpr (h == 0 && k == 4) { PINW(4); PINW(5); }
        wd_issue(job, h);
      }
      SB();
      if constexpr (n == 4) asm volatile("buffer_load_dwordx2 %0, %1, %2, 0 offen sc1" : "=v"(f5v) : "v"(WS_F5 + (unsigned)((fg + 14 * (l & 1)) * 256)), "s"(rWS) : "memory");
      if constexpr (n == 19) {
        asm volatile("s_waitcnt vmcnt(5)\n\ts_barrier" : "+v"(f5v)::"memory");
        if (!__all((((u64)f5v.y << 32) | f5v.x) == tag)) poll((GAS u64*)(wsg + WS_F5 + (fg + 14 * (l & 1)) * 256), l < 2, tag);
        const unsigned so = WS_CS + (unsigned)(fg * 512 + w * 64);
        csv[0] = bload_sc1<0>(hh * 16, rWS, so); csv[1] = bload_sc1<32>(hh * 16, rWS, so);
        csv[2] = bload_sc1<0>(hh * 16, rWS, so + 7168); csv[3] = bload_sc1<32>(hh * 16, rWS, so + 7168);
        SB();
      }
      if constexpr (n == 29) { asm volatile("s_waitcnt vmcnt(2)\n\ts_barrier" : "+v"(csv[0]), "+v"(csv[1]), "+v"(csv[2]), "+v"(csv[3])::"memory"); SB(); }
      if constexpr (n == 47) { waitv<6>(); asm volatile("s_barrier" ::: "memory"); SB(); }
      if constexpr (n == 50) { if (tid == 0) *(volatile LAS int*)(L + L_RS + 7168) = 0; }
      if constexpr (n == 57) { asm volatile("s_barrier" ::: "memory"); SB(); }
      if constexpr (n == 84) { waitv<6>(); SB(); }
      if constexpr (n == 96) {
        waitv<4>();
        asm volatile("s_barrier" ::: "memory");
        if (tid == 0) __hip_atomic_store((GAS u64*)(wsg + WS_F3 + (t3 * 14 + fg) * 256), tag, __ATOMIC_RELAXED, __HIP_MEMORY_SCOPE_AGENT);
        SB();
      }
      if constexpr (n == 27) STAMP(14);
      if constexpr (n == 55) STAMP(18);
      if constexpr (n == 83) STAMP(25);
    });
    STAMP(15);
    const unsigned fo = WS_F3 + (unsigned)(((t64 >> 1) * 14 + (l < 14 ? l : 0)) * 256);
    asm volatile("buffer_load_dwordx2 %0, %1, %2, 0 offen sc1" : "=v"(f3v) : "v"(fo), "s"(rWS) : "memory");
#pragma unroll
    for (int j = 0; j < 8; ++j) {
      et[j] = __builtin_amdgcn_exp2f((ac[1][j] - mean * cs(j)) * ge) * sd + sd;
      eo[j] = (ac[1][j + 8] - mean * cs(j + 8)) * __builtin_amdgcn_rcpf(et[j]);
    }
    epi_st(3);
  }
  STAMP(16);
  SB();
  {
    constexpr int HR = 7;
    asm volatile("s_waitcnt vmcnt(1)" : "+v"(f3v)::"memory");
    if (!__all((((u64)f3v.y << 32) | f3v.x) == tag)) poll((GAS u64*)(wsg + WS_F3 + ((t64 >> 1) * 14 + (l & 15)) * 256), l < 14, tag);
    const unsigned sm = WS_HM + (unsigned)((tb * 112 + w) * 1024);
    v4u hf[HR];
#pragma unroll
    for (int n = 0; n < HR; ++n) hf[n] = bload_sc1<0>(l16, rWS, sm + (unsigned)((8 * n) * 1024));
#pragma unroll
    for (int r = 8; r < 14; ++r) wd_read(r);
    waitv<HR>();
    arrive(L, L_RS + 7168, 8, (GAS u64*)(wsg + WS_F4 + (t3 * 14 + fg) * 256), tag, l);
    STAMP(17);
    unsigned rv0 = (unsigned)(w * 2048) + l16, rv1 = (rv0 ^ 128u) + 65536u;
    asm volatile("" : "+v"(rv0), "+v"(rv1));
    f16v a0, a1;
    static_for<28>([&](auto N_) {
      constexpr int n = N_, t = n / 14, k = n % 14;
      if constexpr (k == 0) {
#pragma unroll
        for (int i = 0; i < 16; ++i) { a0[i] = 0.f; a1[i] = 0.f; }
      }
      waitv1<(k + HR < 14 ? HR - 1 : 13 - k)>(hf[k % HR]);
      a0 = mfma32(W4[2 * k], hf[k % HR], a0); a1 = mfma32(W4[2 * k + 1], hf[k % HR], a1);
      SB();
      if constexpr (k + HR < 14) hf[k % HR] = bload_sc1<0>(l16, rWS, sm + (unsigned)((8 * (k + HR) + 224 * t) * 1024));
      if constexpr (n == 13) {
        if (w == 0) poll((GAS u64*)(wsg + WS_F4 + ((t64 >> 1) * 14 + (l & 15)) * 256), l < 14, tag);
        bar();
        STAMP(19);
#pragma unroll
        for (int i = 0; i < HR; ++i) hf[i] = bload_sc1<0>(l16, rWS, sm + (unsigned)((8 * i + 224) * 1024));
      }
      if constexpr (k == 13) {
#pragma unroll
        for (int j = 0; j < 4; ++j) {
          const f16v& a = (j >> 1) ? a1 : a0;
          const unsigned o = ((j >> 1) ? rv1 : rv0) + ((j & 1) * 2 + t) * 16384;
          *(LAS f4*)(L + o) = f4{a[8 * (j & 1)], a[8 * (j & 1) + 1], a[8 * (j & 1) + 2], a[8 * (j & 1) + 3]};
          *(LAS f4*)(L + o + 1024) = f4{a[8 * (j & 1) + 4], a[8 * (j & 1) + 5], a[8 * (j & 1) + 6], a[8 * (j & 1) + 7]};
        }
        SB();
      }
    });
    STAMP(20);
    f4 o0 = {lo2f(hres.x), hi2f(hres.x), lo2f(hres.y), hi2f(hres.y)}, o1 = {lo2f(hres.z), hi2f(hres.z), lo2f(hres.w), hi2f(hres.w)};
    unsigned rr4 = 8 * rown + lown;
    asm volatile("" : "+v"(o0), "+v"(o1), "+v"(rr4));
    bar();
    f4 pr[16];
#pragma unroll
    for (int s = 0; s < 16; ++s) pr[s] = *(LAS f4*)(L + rr4 + s * 1024);
    SB();
    o0 += ((pr[0] + pr[2]) + (pr[4] + pr[6])) + ((pr[8] + pr[10]) + (pr[12] + pr[14]));
    o1 += ((pr[1] + pr[3]) + (pr[5] + pr[7])) + ((pr[9] + pr[11]) + (pr[13] + pr[15]));
    auto r0 = __builtin_amdgcn_permlane32_swap(pk(o0[0], o0[1]), pk(o1[0], o1[1]), false, false);
    auto r1 = __builtin_amdgcn_permlane32_swap(pk(o0[2], o0[3]), pk(o1[2], o1[3]), false, false);
    const v4u r = {r0[0], r1[0], r0[1], r1[1]};
    *(GAS v4u*)((GAS char*)out + oown) = r;
  }
  STAMP(21);
  STAMP_OUT();
  asm volatile(".Lfb_end:" ::: "memory");
}

extern "C" void kernel_launch(void* const* d_in, const int* in_sizes, int n_in, void* d_out, int out_size, void* d_ws, size_t ws_size,
                              hipStream_t stream) {
  (void)hipFuncSetAttribute((const void*)fused_block, hipFuncAttributeMaxDynamicSharedMemorySize, LDS_BYTES);
  fused_block<<<256, 512, LDS_BYTES, stream>>>((const char*)d_in[0], (const char*)d_in[1], (const char*)d_in[2], (const char*)d_in[4],
                                               (const char*)d_in[5], (const char*)d_in[6], (char*)d_out, (char*)d_ws);
}
```
